```python
import torch

def _softmax(x, axis=-1):
    return torch.softmax(x, dim=axis)
def _gelu(x):
    return torch.nn.functional.gelu(x, approximate="tanh")
def _silu(x):
    return torch.nn.functional.silu(x)
def _sigmoid(x):
    return torch.sigmoid(x)
def _layernorm(x, axis=-1, eps=1e-5):
    m = x.mean(dim=axis, keepdim=True)
    v = x.var(dim=axis, keepdim=True, unbiased=False)
    return (x - m) / torch.sqrt(v + eps)
def _scatter_add(tgt, idx, src, axis=0):
    return tgt.clone().index_add_(axis, idx, src)
def _segment_sum(src, seg, n):
    out = torch.zeros((n,) + src.shape[1:], dtype=src.dtype)
    return out.index_add_(0, seg, src)
def _pad(x, pw):
    flat = tuple(v for pair in reversed(pw) for v in pair)
    return torch.nn.functional.pad(x, flat)

kernel_name = 'parallel_sum__ssm_conv_s4__dense_activation_mish__prompt'

def setup_inputs():

    _w = (lambda a: (lambda t: t.to(torch.bfloat16) if t.is_floating_point() else t)(torch.as_tensor(a)))
    def _A_setup():
        B, L, D, N = 1, 2048, 512, 128
        A_re = -(lambda _x: torch.exp(torch.as_tensor(_x)))((lambda *s: torch.randn(*s, dtype=torch.bfloat16) * 6.4)(D, N))
        A_im = torch.pi * (lambda *s: torch.randn(*s, dtype=torch.bfloat16) * 0.05)(D, N)
        B_re = (lambda *s: torch.randn(*s, dtype=torch.bfloat16) * 0.1)(D, N)
        B_im = (lambda *s: torch.randn(*s, dtype=torch.bfloat16) * 0.1)(D, N)
        C_re = (lambda *s: torch.randn(*s, dtype=torch.bfloat16) * 0.1)(D, N)
        C_im = (lambda *s: torch.randn(*s, dtype=torch.bfloat16) * 0.1)(D, N)
        log_dt = (lambda *s: torch.randn(*s, dtype=torch.bfloat16) * 0.05)(D) * 0.5 - 4.0
        Dskip = (lambda *s: torch.randn(*s, dtype=torch.bfloat16) * 0.05)(D)
        return {
            "u": _w((lambda *s: torch.randn(*s, dtype=torch.bfloat16) * 0.05)(B, L, D)),
            "A_re": _w(A_re),
            "A_im": _w(A_im),
            "B_re": _w(B_re),
            "B_im": _w(B_im),
            "C_re": _w(C_re),
            "C_im": _w(C_im),
            "log_dt": _w(log_dt),
            "Dskip": _w(Dskip),
        }
    _A_in = _A_setup()

    def _B_setup():
        B, S, D, F = 1, 2048, 512, 1792
        return {
            "x": _w((lambda *s: torch.randn(*s, dtype=torch.bfloat16) * 0.05)(B, S, D)),
            "w1": _w((lambda *s: torch.randn(*s, dtype=torch.bfloat16) * 0.8)(D, F) * (D ** -0.5)),
            "b1": _w((lambda *s: torch.randn(*s, dtype=torch.bfloat16) * 0.2)(F)),
            "w2": _w((lambda *s: torch.randn(*s, dtype=torch.bfloat16) * 0.2)(F, D) * (F ** -0.5)),
        }
    _B_in = _B_setup()

    return {
        "A_u": _A_in["u"],
        "A_A_re": _A_in["A_re"],
        "A_A_im": _A_in["A_im"],
        "A_B_re": _A_in["B_re"],
        "A_B_im": _A_in["B_im"],
        "A_C_re": _A_in["C_re"],
        "A_C_im": _A_in["C_im"],
        "A_log_dt": _A_in["log_dt"],
        "A_Dskip": _A_in["Dskip"],
        "B_x": _B_in["x"],
        "B_w1": _B_in["w1"],
        "B_b1": _B_in["b1"],
        "B_w2": _B_in["w2"],
    }

def reference(A_u, A_A_re, A_A_im, A_B_re, A_B_im, A_C_re, A_C_im, A_log_dt, A_Dskip, B_x, B_w1, B_b1, B_w2):
    def _A_ref(u, A_re, A_im, B_re, B_im, C_re, C_im, log_dt, Dskip):
        Bsz, L, D = u.shape
        dt = torch.exp(log_dt)[:, None]
        A = A_re + 1j * A_im
        Bc = B_re + 1j * B_im
        Cc = C_re + 1j * C_im
        logdA = dt * A
        dB = dt * Bc
        l = (lambda x, dt: x.to(dt))(torch.arange(L), A_re.dtype)
        P = torch.exp(logdA[:, :, None] * l[None, None, :])
        K = torch.einsum("dn,dnl,dn->dl", Cc, P, dB).real
        L2 = 2 * L
        Kp = _pad(K, ((0, 0), (0, L)))
        up = (lambda x, *d: x.permute(*d))(u, 0, 2, 1)
        up = _pad(up, ((0, 0), (0, 0), (0, L)))
        Kf = (lambda x, axes=(-2,-1): torch.fft.rfft2(x.float(), dim=axes))(Kp[:, None, :], axes=(-2, -1))
        Uf = (lambda x, axes=(-2,-1): torch.fft.rfft2(x.float(), dim=axes))(up[:, :, None, :], axes=(-2, -1))
        Yf = Uf * Kf[None, :, :, :]
        y = (lambda x, s, axes=(-2,-1): torch.fft.irfft2(x, s=s, dim=axes).to(torch.bfloat16))(Yf, (1, L2), axes=(-2, -1))[:, :, 0, :L]
        y = (lambda x, *d: x.permute(*d))(y, 0, 2, 1) + u * Dskip[None, None, :]
        return y

    def _B_ref(x, w1, b1, w2):
        h = torch.einsum("bsd,df->bsf", x, w1) + b1
        sp = torch.log1p(torch.exp(-torch.abs(h))) + torch.where(h > 0, h, torch.zeros_like(h))
        act = h * torch.tanh(sp)
        return torch.einsum("bsf,fd->bsd", act, w2)

    x = A_u
    xn = _layernorm(x, axis=-1)
    return x + _A_ref(xn, A_A_re, A_A_im, A_B_re, A_B_im, A_C_re, A_C_im, A_log_dt, A_Dskip) + _B_ref(xn, B_w1, B_b1, B_w2)
```

```cpp
#include <hip/hip_runtime.h>
#include <hip/hip_bf16.h>
#include <stdint.h>
typedef unsigned short u16;
__device__ __forceinline__ float bf2f(u16 v){ return __uint_as_float(((unsigned)v)<<16); }
__device__ __forceinline__ u16 f2bf(float f){ unsigned u=__float_as_uint(f); u += 0x7FFF + ((u>>16)&1); return (u16)(u>>16); }
__device__ __forceinline__ float rbf(float f){ return bf2f(f2bf(f)); }
constexpr int L=2048, D=512, N=128, F=1792;

__global__ void ln_k(const u16* x, u16* xn){
  int r=blockIdx.x; int t=threadIdx.x;
  float v[8]; float s=0;
  for(int i=0;i<8;i++){ v[i]=bf2f(x[r*D+t*8+i]); s+=v[i]; }
  for(int o=32;o;o>>=1) s+=__shfl_xor(s,o);
  float m=s/D; float q=0;
  for(int i=0;i<8;i++){ float d=v[i]-m; q+=d*d; }
  for(int o=32;o;o>>=1) q+=__shfl_xor(q,o);
  float mb=rbf(m), vb=rbf(q/D);
  float sd=rbf(sqrtf(rbf(vb+1e-5f)));
  for(int i=0;i<8;i++){ xn[r*D+t*8+i]=f2bf(rbf(v[i]-mb)/sd); }
}
__global__ void kgen_k(const u16* Are,const u16* Aim,const u16* Bre,const u16* Bim,const u16* Cre,const u16* Cim,const u16* logdt,float* K){
  int d=blockIdx.y; int l=blockIdx.x*256+threadIdx.x;
  float dt=rbf(expf(bf2f(logdt[d])));
  float lf=rbf((float)l);
  float acc=0;
  for(int n=0;n<N;n++){
    int i=d*N+n;
    float zr=dt*bf2f(Are[i]), zi=dt*bf2f(Aim[i]);
    float br=dt*bf2f(Bre[i]), bi=dt*bf2f(Bim[i]);
    float cr=bf2f(Cre[i]), ci=bf2f(Cim[i]);
    float gr=cr*br-ci*bi, gi=cr*bi+ci*br;
    float e=expf(zr*lf); float sn,cs; sincosf(zi*lf,&sn,&cs);
    acc+= e*(gr*cs-gi*sn);
  }
  K[d*L+l]=acc;
}
__global__ void conv_k(const float* K,const u16* xn,const u16* Dskip,u16* yA){
  int d=blockIdx.x*64+(threadIdx.x&63); int l=blockIdx.y*4+(threadIdx.x>>6);
  float acc=0;
  for(int j=0;j<=l;j++) acc+=K[d*L+j]*bf2f(xn[(l-j)*D+d]);
  float u=bf2f(xn[l*D+d]);
  yA[l*D+d]=f2bf(rbf(acc)+rbf(u*bf2f(Dskip[d])));
}
__global__ void g1_k(const u16* xn,const u16* w1,const u16* b1,u16* act){
  int f=blockIdx.x*64+(threadIdx.x&63); int l=blockIdx.y*4+(threadIdx.x>>6);
  float acc=0;
  for(int k=0;k<D;k++) acc+=bf2f(xn[l*D+k])*bf2f(w1[k*F+f]);
  float h=rbf(rbf(acc)+bf2f(b1[f]));
  float e=expf(-fabsf(h));
  float num = h>=0 ? 1.f+2.f*e : e*(e+2.f);
  float den = h>=0 ? num+2.f*e*e : num+2.f;
  act[l*F+f]=f2bf(h*num/den);
}
__global__ void g2_k(const u16* act,const u16* w2,const u16* x,const u16* yA,u16* out){
  int d=blockIdx.x*64+(threadIdx.x&63); int l=blockIdx.y*4+(threadIdx.x>>6);
  float acc=0;
  for(int k=0;k<F;k++) acc+=bf2f(act[l*F+k])*bf2f(w2[k*D+d]);
  float o=rbf(bf2f(x[l*D+d])+bf2f(yA[l*D+d]));
  out[l*D+d]=f2bf(o+rbf(acc));
}
extern "C" void kernel_launch(void* const* d_in, const int* in_sizes, int n_in, void* d_out, int out_size, void* d_ws, size_t ws_size, hipStream_t stream){
  const u16* x=(const u16*)d_in[0];
  char* ws=(char*)d_ws;
  u16* xn=(u16*)ws; float* K=(float*)(ws+(4<<20)); u16* yA=(u16*)(ws+(12<<20)); u16* act=(u16*)(ws+(16<<20));
  ln_k<<<L,64,0,stream>>>(x,xn);
  kgen_k<<<dim3(L/256,D),256,0,stream>>>((const u16*)d_in[1],(const u16*)d_in[2],(const u16*)d_in[3],(const u16*)d_in[4],(const u16*)d_in[5],(const u16*)d_in[6],(const u16*)d_in[7],K);
  conv_k<<<dim3(D/64,L/4),256,0,stream>>>(K,xn,(const u16*)d_in[8],yA);
  g1_k<<<dim3(F/64,L/4),256,0,stream>>>(xn,(const u16*)d_in[10],(const u16*)d_in[11],act);
  g2_k<<<dim3(D/64,L/4),256,0,stream>>>(act,(const u16*)d_in[12],x,yA,(u16*)d_out);
}
```

```cpp
#include <hip/hip_runtime.h>
#include <stdint.h>
#include <type_traits>

typedef unsigned short u16;
typedef unsigned int u32;
typedef unsigned long long u64;
typedef short s16x4 __attribute__((ext_vector_type(4)));
typedef _Float16 h16x2 __attribute__((ext_vector_type(2)));
typedef _Float16 h16x8 __attribute__((ext_vector_type(8)));
typedef __bf16 b16x2 __attribute__((ext_vector_type(2)));
typedef __bf16 b16x8 __attribute__((ext_vector_type(8)));
typedef float f32x2 __attribute__((ext_vector_type(2)));
typedef float f32x4 __attribute__((ext_vector_type(4)));
typedef u32 u32x2 __attribute__((ext_vector_type(2)));
typedef u32 u32x4 __attribute__((ext_vector_type(4)));
typedef int i32x4 __attribute__((ext_vector_type(4)));
#define LAS __attribute__((address_space(3)))
#define GAS __attribute__((address_space(1)))
#define RLX_AGENT __ATOMIC_RELAXED, __HIP_MEMORY_SCOPE_AGENT

constexpr int L_ = 2048, D_ = 512, N_ = 128, F_ = 1792;

constexpr u32 WS_STAT = 4096;
constexpr u32 WS_FXN = WS_STAT + 16384;
constexpr u32 WS_FACT = WS_FXN + 2048;
constexpr u32 WS_FY = WS_FACT + 2048;
constexpr u32 WS_XNT = 65536;
constexpr u32 WS_ACT = WS_XNT + 2097152;
constexpr u32 WS_YT = WS_ACT + 7340032;
constexpr u32 WS_END = WS_YT + 2097152;
#define DBG_OFF (16u << 20)

constexpr u32 STAGE = 32768;
constexpr u32 KTAB = 98304;
constexpr u32 KTS = 57 * 528;
constexpr u32 KLIN = 161248;
constexpr u32 KLS = 1296;
constexpr u32 STO = 131072;
constexpr u32 CSO = 131840;
constexpr u32 XT = 135168;
constexpr u32 KRO = 148480;
constexpr u32 KRS = 4576;
constexpr u32 UC = 98304;
constexpr u32 UCS = 4160;
constexpr u32 CRED = 131584;
constexpr u32 YI = 131584;
constexpr u32 LDS_BYTES = 163840;

constexpr int SPIN_CAP = 1 << 16;

__device__ __forceinline__ float bflo(u32 v) { return __uint_as_float(v << 16); }
__device__ __forceinline__ float bfhi(u32 v) { return __uint_as_float(v & 0xffff0000u); }
__device__ __forceinline__ u32 pkbf(float a, float b) {
  f32x2 v = {a, b};
  return __builtin_bit_cast(u32, __builtin_convertvector(v, b16x2));
}
__device__ __forceinline__ u32 pkh(float a, float b) {
  f32x2 v = {a, b};
  return __builtin_bit_cast(u32, __builtin_convertvector(v, h16x2));
}
__device__ __forceinline__ float rbf(float a) { return bflo(pkbf(a, 0.f)); }

__device__ __forceinline__ i32x4 mk_srd(const void* p, u32 bytes) {
  u64 a = (u64)p;
  i32x4 r;
  r[0] = (int)(u32)a;
  r[1] = (int)((u32)(a >> 32) & 0xffffu);
  r[2] = (int)bytes;
  r[3] = 0x00020000;
  return r;
}
__device__ __forceinline__ void dma16(i32x4 srd, u32 voff, u32 soff, u32 dst) {
  u32 keep;
  asm volatile("s_mov_b32 %0, m0\n\ts_mov_b32 m0, %4\n\ts_nop 0\n\tbuffer_load_dwordx4 %1, %2, %3 offen lds\n\ts_mov_b32 m0, %0"
               : "=&s"(keep) : "v"(voff), "s"(srd), "s"(soff), "s"(dst));
}
__device__ __forceinline__ void dma4(i32x4 srd, u32 voff, u32 soff, u32 dst) {
  u32 keep;
  asm volatile("s_mov_b32 %0, m0\n\ts_mov_b32 m0, %4\n\ts_nop 0\n\tbuffer_load_dword %1, %2, %3 offen lds\n\ts_mov_b32 m0, %0"
               : "=&s"(keep) : "v"(voff), "s"(srd), "s"(soff), "s"(dst) : "memory");
}
__device__ __forceinline__ u32 ald4(i32x4 srd, u32 voff) {
  u32 r;
  asm volatile("buffer_load_dword %0, %1, %2, 0 offen" : "=v"(r) : "v"(voff), "s"(srd) : "memory");
  return r;
}
__device__ __forceinline__ u32x2 ald8(i32x4 srd, u32 voff) {
  u32x2 r;
  asm volatile("buffer_load_dwordx2 %0, %1, %2, 0 offen" : "=v"(r) : "v"(voff), "s"(srd) : "memory");
  return r;
}
__device__ __forceinline__ u32x4 ald16(i32x4 srd, u32 voff) {
  u32x4 r;
  asm volatile("buffer_load_dwordx4 %0, %1, %2, 0 offen" : "=v"(r) : "v"(voff), "s"(srd) : "memory");
  return r;
}
#define BAR() asm volatile("s_waitcnt lgkmcnt(0)\n\ts_barrier" ::: "memory")
#define WAITV(n) asm volatile("s_waitcnt vmcnt(" #n ")" ::: "memory")

__device__ __forceinline__ f32x4 mfma_bf(u32x4 a, u32x4 b, f32x4 c) {
  return __builtin_amdgcn_mfma_f32_16x16x32_bf16(__builtin_bit_cast(b16x8, a), __builtin_bit_cast(b16x8, b), c, 0, 0, 0);
}
__device__ __forceinline__ f32x4 mfma_h(u32x4 a, u32x4 b, f32x4 c) {
  return __builtin_amdgcn_mfma_f32_16x16x32_f16(__builtin_bit_cast(h16x8, a), __builtin_bit_cast(h16x8, b), c, 0, 0, 0);
}
__device__ __forceinline__ u32x2 tr_rd(LAS char* p) {
  return __builtin_bit_cast(u32x2, __builtin_amdgcn_ds_read_tr16_b64_v4i16((LAS s16x4*)p));
}
template <int CTRL> __device__ __forceinline__ float dpp_mov(float v) {
  return __builtin_bit_cast(float, __builtin_amdgcn_update_dpp(0, __builtin_bit_cast(int, v), CTRL, 0xf, 0xf, true));
}
__device__ __forceinline__ float rdlane(float v, int i) {
  return __builtin_bit_cast(float, __builtin_amdgcn_readlane(__builtin_bit_cast(int, v), i));
}
__device__ __forceinline__ float wave_max(float v) {
  v = fmaxf(v, dpp_mov<0xB1>(v));
  v = fmaxf(v, dpp_mov<0x4E>(v));
  v = fmaxf(v, dpp_mov<0x141>(v));
  v = fmaxf(v, dpp_mov<0x140>(v));
  return fmaxf(fmaxf(rdlane(v, 0), rdlane(v, 16)), fmaxf(rdlane(v, 32), rdlane(v, 48)));
}
__device__ __forceinline__ float wave_sum(float v) {
  v += dpp_mov<0xB1>(v);
  v += dpp_mov<0x4E>(v);
  v += dpp_mov<0x141>(v);
  v += dpp_mov<0x140>(v);
  return (rdlane(v, 0) + rdlane(v, 16)) + (rdlane(v, 32) + rdlane(v, 48));
}
#define SB() __builtin_amdgcn_sched_barrier(0)
__device__ __forceinline__ u64 probe(GAS u64* p) { return __hip_atomic_load(p, RLX_AGENT); }
__device__ __forceinline__ void wait_flags(GAS u64* p, u64 v, u64 tag) {
#pragma unroll 1
  for (int it = 0; it < SPIN_CAP; ++it) {
    if (__all(v == tag)) break;
    __builtin_amdgcn_s_sleep(1);
    v = __hip_atomic_load(p, RLX_AGENT);
  }
  __builtin_amdgcn_fence(__ATOMIC_ACQUIRE, "wavefront");
}
struct cpx { float re, im; };
__device__ __forceinline__ cpx cmul(cpx a, cpx b) {
  cpx r;
  r.re = fmaf(a.re, b.re, -(a.im * b.im));
  r.im = fmaf(a.re, b.im, a.im * b.re);
  return r;
}

#define STAMP(k)

struct Params {
  const u16 *x, *Are, *Aim, *Bre, *Bim, *Cre, *Cim, *logdt, *dskip, *w1, *b1, *w2;
  u16* out;
  char* ws;
};

__global__ void __launch_bounds__(512, 2) fused_kernel(Params P) {
  extern __shared__ __attribute__((aligned(16))) char smem[];
  LAS char* S = (LAS char*)smem;
  const u32 sbase = (u32)(size_t)S;
  char* ws = P.ws;
  const int t = threadIdx.x, l = t & 63, w = __builtin_amdgcn_readfirstlane(t >> 6);
  const int h = l >> 4, l15 = l & 15;
  const int b = blockIdx.x, xcd = b & 7, rk = b >> 3;
  const int bi = 4 * (xcd >> 1) + (rk >> 3), bj = 8 * (xcd & 1) + (rk & 7);
  const int bp = 4 * xcd + (rk >> 3), bc = rk & 7;
  const int rot = ((rk & 7) + 2 * (rk >> 3)) & 7;
  const int wm = w & 3, wn = w >> 2;
  const int ch = 2 * b + (w >> 2);
  STAMP(0);

  const __amdgpu_buffer_rsrc_t rs_ws = __builtin_amdgcn_make_buffer_rsrc((void*)ws, 0, WS_END, 0x00020000);
  const __amdgpu_buffer_rsrc_t rs_x = __builtin_amdgcn_make_buffer_rsrc((void*)P.x, 0, L_ * D_ * 2, 0x00020000);
  const __amdgpu_buffer_rsrc_t rs_out = __builtin_amdgcn_make_buffer_rsrc((void*)P.out, 0, L_ * D_ * 2, 0x00020000);
  const i32x4 sx = mk_srd(P.x, L_ * D_ * 2), sw1 = mk_srd(P.w1, D_ * F_ * 2), sw2 = mk_srd(P.w2, D_ * F_ * 2);

  const u64 tag = *(const u64*)ws + 1ull;
  const u32 ldt2 = *(const u32*)(P.logdt + 2 * b);
  const u32 dsk2 = *(const u32*)(P.dskip + 2 * b);

  u32 p_ar, p_ai, p_br, p_bi, p_cr, p_ci;
  u32x4 srow, xsub;
  u32x2 b1v[4];
  {
    const u32 po = ch * 256 + 4 * l;
    p_ar = ald4(mk_srd(P.Are, D_ * N_ * 2), po);
    p_ai = ald4(mk_srd(P.Aim, D_ * N_ * 2), po);
    p_br = ald4(mk_srd(P.Bre, D_ * N_ * 2), po);
    p_bi = ald4(mk_srd(P.Bim, D_ * N_ * 2), po);
    p_cr = ald4(mk_srd(P.Cre, D_ * N_ * 2), po);
    p_ci = ald4(mk_srd(P.Cim, D_ * N_ * 2), po);
    srow = ald16(sx, (128 * bi + 8 * bj + w) * 1024 + 16 * l);
    xsub = ald16(sx, (128 * bi + (t >> 2)) * 1024 + bj * 64 + (t & 3) * 16);
    const i32x4 sb1 = mk_srd(P.b1, F_ * 2);
#pragma unroll
    for (int f = 0; f < 4; ++f) b1v[f] = ald8(sb1, bj * 224 + (16 * (4 * wn + f) + 4 * h) * 2);
  }
  const u32 xo0 = (128 * bi + 8 * w + (l >> 3)) * 1024 + (((l & 7) ^ (l >> 3)) * 16);
  const u32 xo1 = xo0 + 64 * 1024;
  u32 wo0, wo1;
  {
    int g0 = w * 64 + l, g1 = (w + 8) * 64 + l;
    int pos0 = g0 / 14, c0 = g0 % 14, pos1 = g1 / 14, c1 = g1 % 14;
    int k0 = (pos0 & ~12) | ((pos0 & 4) << 1) | ((pos0 & 8) >> 1);
    int k1 = (pos1 & ~12) | ((pos1 & 4) << 1) | ((pos1 & 8) >> 1);
    wo0 = k0 * (F_ * 2) + bj * 224 + c0 * 16;
    wo1 = (w < 6) ? k1 * (F_ * 2) + bj * 224 + c1 * 16 : 0x7ff00000u;
  }
#define ISSUE_PIECE(c, n)                                                                        \
  do {                                                                                           \
    const u32 sb_ = sbase + ((c) & 3) * STAGE;                                                   \
    const u32 kc_ = ((c) + rot) & 7;                                                             \
    if ((n) == 0) dma16(sx, xo0, kc_ * 128, sb_ + w * 1024);                                     \
    if ((n) == 1) dma16(sx, xo1, kc_ * 128, sb_ + (w + 8) * 1024);                               \
    if ((n) == 2) dma16(sw1, wo0, kc_ * (64 * F_ * 2), sb_ + 16384 + w * 1024);                  \
    if ((n) == 3) dma16(sw1, wo1, kc_ * (64 * F_ * 2), sb_ + 16384 + (w + 8) * 1024);            \
  } while (0)
#define ISSUE_STAGE(c) do { ISSUE_PIECE(c, 0); ISSUE_PIECE(c, 1); ISSUE_PIECE(c, 2); ISSUE_PIECE(c, 3); } while (0)
  u32 w2o;
  {
    const int rr = l >> 3, slot = l & 7;
    const int dt = (slot >> 1) ^ ((rr >> 1) & 3);
    w2o = (224 * w + rr) * 1024 + bc * 128 + (2 * dt + (slot & 1)) * 16;
  }
#define W2TILE(ks, hf) ((ks) < 3 ? 2 * (ks) + (hf) : ((ks) == 3 ? ((hf) ? 13 : 6) : 7 + 2 * ((ks) - 4) + (hf)))
#define ISSUE_W2(p, n) dma16(sw2, w2o, (W2TILE(p, (n) >> 1) * 16 + ((n) & 1) * 8) * 1024, sbase + ((p) & 3) * STAGE + 4096 * w + (n) * 1024)
  ISSUE_STAGE(0);
  ISSUE_STAGE(1);
  ISSUE_STAGE(2);
  WAITV(12);
  asm volatile("" : "+v"(p_ar), "+v"(p_ai), "+v"(p_br), "+v"(p_bi), "+v"(p_cr), "+v"(p_ci), "+v"(srow), "+v"(xsub));
  asm volatile("" : "+v"(b1v[0]), "+v"(b1v[1]), "+v"(b1v[2]), "+v"(b1v[3]));
  STAMP(1);

  {
    float e[8];
#pragma unroll
    for (int q = 0; q < 4; ++q) { e[2 * q] = bflo(srow[q]); e[2 * q + 1] = bfhi(srow[q]); }
    float s = ((e[0] + e[1]) + (e[2] + e[3])) + ((e[4] + e[5]) + (e[6] + e[7]));
    s = wave_sum(s);
    const float mean = s * (1.0f / D_);
    float ss = 0;
#pragma unroll
    for (int q = 0; q < 8; ++q) { float d = e[q] - mean; ss = fmaf(d, d, ss); }
    ss = wave_sum(ss);
    const float vb = rbf(ss * (1.0f / D_));
    const float sd = sqrtf(rbf(vb + 1e-5f));
    const u32 pay = pkbf(mean, sd);
    if (l == 0) __hip_atomic_store((GAS u64*)(ws + WS_STAT + (128 * bi + 8 * bj + w) * 8), ((u64)(u32)tag << 32) | pay, RLX_AGENT);
  }

  float yscale, gmax;
  {
    const float dt = rbf(expf((w >> 2) ? bfhi(ldt2) : bflo(ldt2)));
    const float LOG2E = 1.4426950408889634f, INV2PI = 0.15915494309189535f;
    const bool hm = w & 1;
    cpx cb0, cb1;
    {
      float b_r = dt * bflo(p_br), b_i = dt * bflo(p_bi);
      cb0 = cmul(cpx{bflo(p_cr), bflo(p_ci)}, cpx{b_r, b_i});
      b_r = dt * bfhi(p_br); b_i = dt * bfhi(p_bi);
      cb1 = cmul(cpx{bfhi(p_cr), bfhi(p_ci)}, cpx{b_r, b_i});
    }
    const float z0 = dt * bflo(p_ar) * LOG2E, z1 = dt * bfhi(p_ar) * LOG2E;
    gmax = fmaxf(wave_max(fmaxf(z0, z1)), 0.f);
    const float cbmax = wave_max(fmaxf(fmaxf(fabsf(cb0.re), fabsf(cb0.im)), fmaxf(fabsf(cb1.re), fabsf(cb1.im))));
    const float icb = cbmax > 0.f ? 1.0f / cbmax : 0.f;
    yscale = cbmax * __builtin_amdgcn_exp2f(gmax * 2048.f) * (1.0f / 64.f);
    const float zr = fmaxf((hm ? z1 : z0) - gmax, -1e30f);
    const float zi = dt * (hm ? bfhi(p_ai) : bflo(p_ai)) * INV2PI;
    auto cexp = [&](float a) {
      float mg = __builtin_amdgcn_exp2f(zr * a);
      float ph = __builtin_amdgcn_fractf(zi * a);
      return cpx{mg * __builtin_amdgcn_cosf(ph), mg * __builtin_amdgcn_sinf(ph)};
    };
    LAS char* tb = S + KTAB + (w >> 2) * KTS + ((w & 1) * 64 + l) * 4;
    auto put = [&](int row, cpx v) { *(LAS u32*)(tb + row * 528) = pkh(v.re, v.im); };
    const cpx e128 = cexp(128.f);
    if (((w >> 1) & 1) == 0) {
      const cpx e1 = cexp(1.f), e8 = cexp(8.f);
      cpx wv = hm ? cb1 : cb0;
      wv.re *= icb; wv.im *= -icb;
      const cpx e8c = cpx{e8.re, -e8.im};
      cpx lo = cpx{1.f, 0.f}, g = e128;
#pragma unroll
      for (int q = 0; q < 16; ++q) {
        put(q, wv);
        if (q < 15) wv = cmul(wv, e8c);
        if (q < 8) {
          put(16 + q, lo);
          put(24 + q, g);
          if (q < 7) { lo = (q == 0) ? e1 : cmul(lo, e1); g = cmul(g, e1); }
        }
      }
    } else {
      const cpx e2 = cexp(2.f);
      const cpx e4 = cmul(e2, e2), e6 = cmul(e4, e2);
      cpx hv = cmul(e128, e128);
#pragma unroll
      for (int a = 2; a <= 16; ++a) {
        if (a < 4) {
          put(16 + 16 + (a - 2) * 4 + 0, hv);
          put(16 + 16 + (a - 2) * 4 + 1, cmul(hv, e2));
          put(16 + 16 + (a - 2) * 4 + 2, cmul(hv, e4));
          put(16 + 16 + (a - 2) * 4 + 3, cmul(hv, e6));
        } else if (a < 8) {
          put(16 + 24 + (a - 4) * 2 + 0, hv);
          put(16 + 24 + (a - 4) * 2 + 1, cmul(hv, e4));
        } else {
          put(16 + 32 + (a - 8), hv);
        }
        if (a < 16) hv = cmul(hv, e128);
      }
    }
  }
  GAS u64* stat_p = (GAS u64*)(ws + WS_STAT + (128 * bi + (t >> 2)) * 8);
  u64 stat_v = probe(stat_p);
  BAR();
  STAMP(2);
  if ((w & 3) < 3) {
    const int tile = w & 3;
    LAS char* fa = S + KTAB + (w >> 2) * KTS + l15 * 528 + h * 16;
    LAS char* fb = fa + (16 + 16 * tile) * 528;
    f32x4 ka = {0, 0, 0, 0}, kb = {0, 0, 0, 0};
#pragma unroll
    for (int ts = 0; ts < 8; ts += 2) {
      ka = mfma_h(*(LAS u32x4*)(fa + ts * 64), *(LAS u32x4*)(fb + ts * 64), ka);
      kb = mfma_h(*(LAS u32x4*)(fa + ts * 64 + 64), *(LAS u32x4*)(fb + ts * 64 + 64), kb);
    }
    ka += kb;
    const int rho = 16 * tile + l15;
    int e, lv;
    if (rho < 16) { e = 0; lv = (rho >> 3) * 128 + (rho & 7); }
    else if (rho < 24) { e = 1; lv = 256 + ((rho - 16) >> 2) * 128 + 2 * ((rho - 16) & 3); }
    else if (rho < 32) { e = 2; lv = 512 + ((rho - 24) >> 1) * 128 + 4 * ((rho - 24) & 1); }
    else { e = 3; lv = 1024 + (rho - 32) * 128; }
    lv += 32 * h;
    const int idx = (lv >> e) + 128 * e, step = 8 >> e;
    float sc[4] = {64.f, 64.f, 64.f, 64.f};
    if (gmax > 0.f) {
#pragma unroll
      for (int r = 0; r < 4; ++r) sc[r] = 64.f * __builtin_amdgcn_exp2f(gmax * (float)(lv + 8 * r - 2048));
    }
#pragma unroll
    for (int r = 0; r < 4; ++r) {
      const int ix = idx + r * step;
      if (ix <= 640) *(LAS _Float16*)(S + KLIN + (w >> 2) * KLS + ix * 2) = (_Float16)(ka[r] * sc[r]);
    }
  }
  {
    const int row = t >> 2;
#pragma unroll 1
    for (int it = 0; it < SPIN_CAP; ++it) {
      if (__all((u32)(stat_v >> 32) == (u32)tag)) break;
      __builtin_amdgcn_s_sleep(1);
      stat_v = probe(stat_p);
    }
    const u32 pay = (u32)stat_v;
    const float mb = bflo(pay), rsd = 1.0f / bfhi(pay);
    u32 xn[4];
#pragma unroll
    for (int q = 0; q < 4; ++q) {
      u32 dd = pkbf(bflo(xsub[q]) - mb, bfhi(xsub[q]) - mb);
      xn[q] = pkbf(bflo(dd) * rsd, bfhi(dd) * rsd);
    }
    WAITV(0);
    BAR();
    STAMP(3);
    ISSUE_STAGE(3);
    if ((t & 3) == 0) *(LAS u32*)(S + STO + row * 4) = pay;
#pragma unroll
    for (int q = 0; q < 4; ++q) {
      const int c0 = (t & 3) * 8 + 2 * q;
      *(LAS u16*)(S + XT + c0 * 272 + row * 2) = (u16)xn[q];
      *(LAS u16*)(S + XT + (c0 + 1) * 272 + row * 2) = (u16)(xn[q] >> 16);
    }
  }

  f32x4 acc[4][2], accs[4];
#pragma unroll
  for (int f = 0; f < 4; ++f) { acc[f][0] = f32x4{0, 0, 0, 0}; acc[f][1] = f32x4{0, 0, 0, 0}; accs[f] = f32x4{0, 0, 0, 0}; }
  {
    const u32 xa = (32 * wm + l15) * 128;
    LAS char* xp0 = S + xa + ((h ^ (l & 7)) * 16);
    LAS char* xp1 = S + xa + (((4 + h) ^ (l & 7)) * 16);
    LAS char* wp = S + 16384 + (16 * (h >> 1) + 4 * (h & 1) + (l15 >> 2)) * 224 + (l & 3) * 8 + wn * 128;
    const u32x4 ones = {0x3f803f80u, 0x3f803f80u, 0x3f803f80u, 0x3f803f80u};
    auto g1 = [&](auto NFc) {
      constexpr int NF = decltype(NFc)::value;
      u32x4 xf[2][2], wf[2][NF];
      auto rd = [&](int s) {
        const u32 so = ((s >> 1) & 3) * STAGE;
        LAS char* xp = (s & 1) ? xp1 : xp0;
        xf[s & 1][0] = *(LAS u32x4*)(xp + so);
        xf[s & 1][1] = *(LAS u32x4*)(xp + so + 2048);
#pragma unroll
        for (int f = 0; f < NF; ++f) {
          u32x2 lo = tr_rd(wp + so + (s & 1) * 32 * 224 + f * 32);
          u32x2 hi = tr_rd(wp + so + (s & 1) * 32 * 224 + 8 * 224 + f * 32);
          wf[s & 1][f] = u32x4{lo[0], lo[1], hi[0], hi[1]};
        }
      };
      rd(0);
#pragma unroll
      for (int s = 0; s < 16; ++s) {
        const int c = (s + 1) >> 1;
        if (s & 1) {
          if (c < 8) {
            if (c >= 3) WAITV(8);
            BAR();
            if (c == 4 && t == 0) __hip_atomic_store((GAS u64*)(ws + WS_FXN + (bj * 16 + bi) * 8), tag, RLX_AGENT);
          }
        }
        if (s < 15) rd(s + 1);
        if (s == 1) {
          u32x4 v = *(LAS u32x4*)(S + XT + (t >> 4) * 272 + (t & 15) * 16);
          __builtin_amdgcn_raw_buffer_store_b128(v, rs_ws, WS_XNT + ((32 * bj + (t >> 4)) * 2048 + 128 * bi + (t & 15) * 8) * 2, 0, 16);
        }
        if (s == 4) {
          const int tt = t & 255;
          LAS char* kl = S + KLIN + (w >> 2) * KLS;
          u32 o[4];
#pragma unroll
          for (int q = 0; q < 4; ++q) {
            const int j0 = 2047 - (8 * tt + 2 * q), j1 = j0 - 1;
            const u32 bits = pkbf((float)j0, (float)j1);
            const int i0 = j0 < 128 ? j0 : (int)(bits & 0xffff) - 0x4280;
            const int i1 = j1 < 128 ? j1 : (int)(bits >> 16) - 0x4280;
            o[q] = (u32)*(LAS u16*)(kl + i0 * 2) | ((u32)*(LAS u16*)(kl + i1 * 2) << 16);
          }
          *(LAS u32x4*)(S + KRO + (w >> 2) * KRS + tt * 16) = u32x4{o[0], o[1], o[2], o[3]};
          if (tt < 30) *(LAS u32x4*)(S + KRO + (w >> 2) * KRS + 4096 + tt * 16) = u32x4{0, 0, 0, 0};
        }
        const int pc = (s & 1) ? c : c;
        const int n0 = (s & 1) ? 0 : 2;
#pragma unroll
        for (int f = 0; f < NF; ++f) {
          SB();
          acc[f][0] = mfma_bf(wf[s & 1][f], xf[s & 1][0], acc[f][0]);
          acc[f][1] = mfma_bf(wf[s & 1][f], xf[s & 1][1], acc[f][1]);
          SB();
          if (f < 2 && s >= 1 && s <= 14) {
            if (pc <= 4) ISSUE_PIECE(pc + 3, n0 + f);
            else ISSUE_W2(pc - 5, n0 + f);
          }
        }
        SB();
        if (wm == (s & 3)) {
#pragma unroll
          for (int f = 0; f < NF; ++f) accs[f] = mfma_bf(wf[s & 1][f], ones, accs[f]);
        }
      }
      if (l15 == 0) {
#pragma unroll
        for (int f = 0; f < NF; ++f) *(LAS f32x4*)(S + CSO + wm * 448 + (16 * (4 * wn + f) + 4 * h) * 4) = accs[f];
      }
    };
    if (wn == 0) g1(std::integral_constant<int, 4>{}); else g1(std::integral_constant<int, 3>{});
  }
  BAR();
  STAMP(4);

  GAS u64* fxn_p = (GAS u64*)(ws + WS_FXN + ((ch >> 5) * 16 + l15) * 8);
  const int cc = w >> 2, cg = (w >> 1) & 1, cr_ = w & 1;
  const int T0 = cg ? (cr_ ? 5 : 1) : (cr_ ? 4 : 0), T1 = cg ? (cr_ ? 6 : 2) : (cr_ ? 7 : 3);
  u32x4 ua; u32x2 ub, us0, us1;
  u32 w2a[4];
  {
    const int q = l15 >> 2, p = l & 3, x = (2 * h + (q >> 1)) & 3;
#pragma unroll
    for (int dt = 0; dt < 4; ++dt) w2a[dt] = 4096 * w + (4 * h + q) * 128 + ((dt ^ x) * 32) + (p >> 1) * 16 + (p & 1) * 8;
  }
  u32x4 wr1[4][4];
  auto w2_to_regs = [&](int ks) {
#pragma unroll
    for (int dt = 0; dt < 4; ++dt) {
      u32x2 lo = tr_rd(S + w2a[dt] + ks * STAGE);
      u32x2 hi = tr_rd(S + w2a[dt] + ks * STAGE + 2048);
      wr1[ks][dt] = u32x4{lo[0], lo[1], hi[0], hi[1]};
    }
  };
  {
    u64 fxn_v = probe(fxn_p);
    float rs[2], tm[2];
#pragma unroll
    for (int mt = 0; mt < 2; ++mt) {
      u32 pay = *(LAS u32*)(S + STO + (32 * wm + 16 * mt + l15) * 4);
      rs[mt] = 1.0f / bfhi(pay);
      tm[mt] = -bflo(pay) * rs[mt];
    }
    u32x2 pk[4][2];
#pragma unroll
    for (int f = 0; f < 4; ++f) {
      if (f < 3 || wn == 0) {
        const int fo = 16 * (4 * wn + f) + 4 * h;
        f32x4 cs = *(LAS f32x4*)(S + CSO + fo * 4);
#pragma unroll
        for (int p = 1; p < 4; ++p) cs += *(LAS f32x4*)(S + CSO + p * 448 + fo * 4);
        u32x2 bv = b1v[f];
        float bia[4] = {bflo(bv[0]), bfhi(bv[0]), bflo(bv[1]), bfhi(bv[1])};
#pragma unroll
        for (int mt = 0; mt < 2; ++mt) {
          float a[4];
#pragma unroll
          for (int r = 0; r < 4; ++r) {
            float hh = fmaf(acc[f][mt][r], rs[mt], fmaf(tm[mt], cs[r], bia[r]));
            float s1 = __builtin_amdgcn_exp2f(hh * 1.4426950408889634f) + 1.f;
            float rr = __builtin_amdgcn_rcpf(fmaf(s1, s1, 1.f));
            a[r] = fmaf(hh * rr, -2.f, hh);
          }
          pk[f][mt] = u32x2{pkbf(a[0], a[1]), pkbf(a[2], a[3])};
          if (f < 2) { SB(); ISSUE_W2(3, 2 * f + mt); SB(); }
        }
      }
    }
    const int jp = bj & 1;
#pragma unroll
    for (int mt = 0; mt < 2; ++mt) {
      const u32 base = WS_ACT + ((8 * bi + 2 * wm + mt) * 56 + 7 * (bj >> 1)) * 1024 + l * 16;
      const u32 k0 = (jp ? 4 : 0) + 2 * wn;
      __builtin_amdgcn_raw_buffer_store_b128(u32x4{pk[0][mt][0], pk[0][mt][1], pk[1][mt][0], pk[1][mt][1]}, rs_ws, base + k0 * 1024, 0, 16);
      if (wn == 0)
        __builtin_amdgcn_raw_buffer_store_b128(u32x4{pk[2][mt][0], pk[2][mt][1], pk[3][mt][0], pk[3][mt][1]}, rs_ws, base + (k0 + 1) * 1024, 0, 16);
      else
        __builtin_amdgcn_raw_buffer_store_b64(pk[2][mt], rs_ws, base + 3 * 1024 + jp * 8, 0, 16);
    }
    STAMP(5);
    wait_flags(fxn_p, fxn_v, tag);
    {
      const u32 uo = WS_XNT + (ch * 2048 + (t & 255) * 8) * 2;
      ua = __builtin_amdgcn_raw_buffer_load_b128(rs_ws, uo, 0, 16);
      ub = __builtin_amdgcn_raw_buffer_load_b64(rs_ws, uo + 16, 0, 16);
      us0 = __builtin_amdgcn_raw_buffer_load_b64(rs_ws, WS_XNT + (ch * 2048 + 256 * T0 + 16 * l15 + 4 * h) * 2, 0, 16);
      us1 = __builtin_amdgcn_raw_buffer_load_b64(rs_ws, WS_XNT + (ch * 2048 + 256 * T1 + 16 * l15 + 4 * h) * 2, 0, 16);
    }
    WAITV(4);
    w2_to_regs(0); w2_to_regs(1); w2_to_regs(2); w2_to_regs(3);
  }

  GAS u64* fact_p = (GAS u64*)(ws + WS_FACT + ((bp >> 1) * 16 + 2 * w + (l & 1)) * 8);
  u64 fact_v;
  {
    const int tt = t & 255;
    BAR();
    if (t == 0) __hip_atomic_store((GAS u64*)(ws + WS_FACT + (bi * 16 + bj) * 8), tag, RLX_AGENT);
    u32 d[6], o[5];
#pragma unroll
    for (int q = 0; q < 4; ++q) d[q] = pkh(bflo(ua[q]), bfhi(ua[q]));
#pragma unroll
    for (int q = 0; q < 2; ++q) d[4 + q] = pkh(bflo(ub[q]), bfhi(ub[q]));
#pragma unroll
    for (int q = 0; q < 5; ++q) o[q] = __builtin_amdgcn_alignbit(d[q + 1], d[q], 16);
    LAS char* ucb = S + UC + cc * 4 * UCS + (tt + 2) * 16;
    *(LAS u32x4*)(ucb) = u32x4{d[0], d[1], d[2], d[3]};
    *(LAS u32x4*)(ucb + UCS) = u32x4{o[0], o[1], o[2], o[3]};
    *(LAS u32x4*)(ucb + 2 * UCS) = u32x4{d[1], d[2], d[3], d[4]};
    *(LAS u32x4*)(ucb + 3 * UCS) = u32x4{o[1], o[2], o[3], o[4]};
    if (tt == 0) {
      LAS char* z = S + UC + cc * 4 * UCS;
#pragma unroll
      for (int s = 0; s < 4; ++s) *(LAS u32x4*)(z + s * UCS) = u32x4{0, 0, 0, 0};
      *(LAS u32x4*)(z + 16) = u32x4{0, 0, 0, 0};
      *(LAS u32x4*)(z + UCS + 16) = u32x4{0, 0, 0, d[0] << 16};
      *(LAS u32x4*)(z + 2 * UCS + 16) = u32x4{0, 0, 0, d[0]};
      *(LAS u32x4*)(z + 3 * UCS + 16) = u32x4{0, 0, d[0] << 16, o[0]};
    }
    BAR();
    STAMP(6);
    f32x4 ca[4];
#pragma unroll
    for (int q = 0; q < 4; ++q) ca[q] = f32x4{0, 0, 0, 0};
    const int v = l15 + 1, sft = v & 3;
    LAS char* ubase = S + UC + (cc * 4 + sft) * UCS + (8 * h + v - sft) * 2 + cr_ * 4 * 64;
    LAS char* ubase2 = ubase + 8;
    asm volatile("" : "+v"(ubase2));
    LAS char* kbase = S + KRO + cc * KRS + (2032 - 1792 - 16 * l15 + 8 * h) * 2 + cr_ * 4 * 64;
    auto cv = [&](auto Gc) {
      constexpr int G = decltype(Gc)::value;
      constexpr int TA = G ? 1 : 0, TB = G ? 2 : 3, TC = G ? 5 : 4, TD = G ? 6 : 7;
#pragma unroll
      for (int rho = 0; rho < 4; ++rho) {
        u32x4 U[TD + 1], Kf[TD + 1];
#pragma unroll
        for (int i = 0; i <= TD; ++i) {
          Kf[TD - i] = *(LAS u32x4*)(kbase + rho * 64 + (7 - (TD - i)) * 512);
          u32x2 lo = *(LAS u32x2*)(ubase + rho * 64 + i * 512);
          u32x2 hi = *(LAS u32x2*)(ubase2 + rho * 64 + i * 512);
          U[i] = u32x4{lo[0], lo[1], hi[0], hi[1]};
        }
#pragma unroll
        for (int i = 0; i <= TD; ++i) {
          ca[3] = mfma_h(U[i], Kf[TD - i], ca[3]);
          if (i >= TD - TC) ca[2] = mfma_h(U[i - (TD - TC)], Kf[TD - i], ca[2]);
          if (i >= TD - TB) ca[1] = mfma_h(U[i - (TD - TB)], Kf[TD - i], ca[1]);
          if (i >= TD - TA) ca[0] = mfma_h(U[i - (TD - TA)], Kf[TD - i], ca[0]);
          if (rho < 3 && i >= 2 && i < 6) ISSUE_W2(4 + rho, i - 2);
        }
      }
    };
    if (cg == 0) cv(std::integral_constant<int, 0>{}); else cv(std::integral_constant<int, 1>{});
    f32x4 s0 = cr_ ? ca[0] : ca[2], s1 = cr_ ? ca[1] : ca[3];
    f32x4 k0 = cr_ ? ca[2] : ca[0], k1 = cr_ ? ca[3] : ca[1];
    *(LAS f32x4*)(S + CRED + w * 2048 + l * 16) = s0;
    *(LAS f32x4*)(S + CRED + w * 2048 + 1024 + l * 16) = s1;
    BAR();
    STAMP(7);
    k0 += *(LAS f32x4*)(S + CRED + (w ^ 1) * 2048 + l * 16);
    k1 += *(LAS f32x4*)(S + CRED + (w ^ 1) * 2048 + 1024 + l * 16);
    const float dsk = cc ? bfhi(dsk2) : bflo(dsk2);
    auto fin = [&](f32x4 a, u32x2 us) {
      u32 y01 = pkbf(a[0] * yscale, a[1] * yscale), y23 = pkbf(a[2] * yscale, a[3] * yscale);
      u32 t01 = pkbf(bflo(us[0]) * dsk, bfhi(us[0]) * dsk), t23 = pkbf(bflo(us[1]) * dsk, bfhi(us[1]) * dsk);
      return u32x2{pkbf(bflo(y01) + bflo(t01), bfhi(y01) + bfhi(t01)), pkbf(bflo(y23) + bflo(t23), bfhi(y23) + bfhi(t23))};
    };
    __builtin_amdgcn_raw_buffer_store_b64(fin(k0, us0), rs_ws, WS_YT + (ch * 2048 + 256 * T0 + 16 * l15 + 4 * h) * 2, 0, 16);
    __builtin_amdgcn_raw_buffer_store_b64(fin(k1, us1), rs_ws, WS_YT + (ch * 2048 + 256 * T1 + 16 * l15 + 4 * h) * 2, 0, 16);
    fact_v = probe(fact_p);
    WAITV(0);
    BAR();
    if (t == 0) __hip_atomic_store((GAS u64*)(ws + WS_FY + b * 8), tag, RLX_AGENT);
  }

  {
    wait_flags(fact_p, fact_v, tag);
    STAMP(8);
    f32x4 ac[4][4];
#pragma unroll
    for (int dt = 0; dt < 4; ++dt)
#pragma unroll
      for (int lt = 0; lt < 4; ++lt) ac[dt][lt] = f32x4{0, 0, 0, 0};
    const u32 ao = WS_ACT + (4 * bp * 56 + 7 * w) * 1024 + l * 16;
    u32x4 af[7][4];
#pragma unroll
    for (int ks = 0; ks < 7; ++ks)
#pragma unroll
      for (int lt = 0; lt < 4; ++lt) af[ks][lt] = __builtin_amdgcn_raw_buffer_load_b128(rs_ws, ao + (lt * 56 + ks) * 1024, 0, 16);
    u32x4 xres = __builtin_amdgcn_raw_buffer_load_b128(rs_x, (64 * bp + (t >> 3)) * 1024 + bc * 128 + (t & 7) * 16, 0, 0);
    GAS u64* fy_p = (GAS u64*)(ws + WS_FY + (32 * bc + (l & 31)) * 8);
    u64 fy_v = probe(fy_p);
    __builtin_amdgcn_sched_barrier(0);
    u32x4 yv;
#pragma unroll
    for (int ks = 0; ks < 7; ++ks) {
      u32x4 wf[4];
#pragma unroll
      for (int dt = 0; dt < 4; ++dt) {
        if (ks < 4) wf[dt] = wr1[ks][dt];
        else {
          u32x2 lo = tr_rd(S + w2a[dt] + (ks - 4) * STAGE);
          u32x2 hi = tr_rd(S + w2a[dt] + (ks - 4) * STAGE + 2048);
          wf[dt] = u32x4{lo[0], lo[1], hi[0], hi[1]};
        }
      }
      if (ks == 6) {
        wait_flags(fy_p, fy_v, tag);
        yv = __builtin_amdgcn_raw_buffer_load_b128(rs_ws, WS_YT + ((64 * bc + (t >> 3)) * 2048 + 64 * bp + (t & 7) * 8) * 2, 0, 16);
      }
#pragma unroll
      for (int lt = 0; lt < 4; ++lt)
#pragma unroll
        for (int dt = 0; dt < 4; ++dt) ac[dt][lt] = mfma_bf(wf[dt], af[ks][lt], ac[dt][lt]);
    }
    STAMP(9);
    BAR();
#pragma unroll
    for (int dt = 0; dt < 4; ++dt) {
      const int sg = ((2 * dt + (h >> 1)) & 3) * 4;
#pragma unroll
      for (int lt = 0; lt < 4; ++lt)
        *(LAS f32x4*)(S + 16384 * w + (dt * 4 + lt) * 1024 + h * 256 + ((l15 ^ sg) * 16)) = ac[dt][lt];
    }
    *(LAS u32x4*)(S + YI + (t >> 3) * 128 + (((t & 7) ^ (t >> 6)) * 16)) = yv;
    BAR();
    STAMP(10);
    {
      const int lrow = t >> 3, g = t & 7;
      const u32 po = ((g >> 1) * 4 + (lrow >> 4)) * 1024 + (2 * (g & 1)) * 256 + (((lrow & 15) ^ ((g & 3) * 4)) * 16);
      f32x4 s0 = {0, 0, 0, 0}, s1 = {0, 0, 0, 0};
#pragma unroll
      for (int p = 0; p < 8; ++p) {
        s0 += *(LAS f32x4*)(S + 16384 * p + po);
        s1 += *(LAS f32x4*)(S + 16384 * p + po + 256);
      }
      float yb[8] = {s0[0], s0[1], s0[2], s0[3], s1[0], s1[1], s1[2], s1[3]};
      u32 o[4];
#pragma unroll
      for (int q = 0; q < 4; ++q) {
        u32 y0 = *(LAS u16*)(S + YI + (8 * g + 2 * q) * 128 + (((lrow >> 3) ^ g) * 16) + (lrow & 7) * 2);
        u32 y1 = *(LAS u16*)(S + YI + (8 * g + 2 * q + 1) * 128 + (((lrow >> 3) ^ g) * 16) + (lrow & 7) * 2);
        u32 a = pkbf(bflo(xres[q]) + bflo(y0), bfhi(xres[q]) + bflo(y1));
        u32 bq = pkbf(yb[2 * q], yb[2 * q + 1]);
        o[q] = pkbf(bflo(a) + bflo(bq), bfhi(a) + bfhi(bq));
      }
      __builtin_amdgcn_raw_buffer_store_b128(u32x4{o[0], o[1], o[2], o[3]}, rs_out, (64 * bp + lrow) * 1024 + bc * 128 + g * 16, 0, 0);
    }
  }
  if (b == 0 && w == 0) {
#pragma unroll 1
    for (int q = 0; q < 4; ++q) {
      GAS u64* p = (GAS u64*)(ws + WS_FXN + (q * 64 + l) * 8);
      wait_flags(p, probe(p), tag);
    }
    if (l == 0) __hip_atomic_store((GAS u64*)ws, tag, RLX_AGENT);
  }
  STAMP(11);
}

extern "C" void kernel_launch(void* const* d_in, const int* in_sizes, int n_in, void* d_out, int out_size, void* d_ws,
                              size_t ws_size, hipStream_t stream) {
  Params P;
  P.x = (const u16*)d_in[0];
  P.Are = (const u16*)d_in[1];
  P.Aim = (const u16*)d_in[2];
  P.Bre = (const u16*)d_in[3];
  P.Bim = (const u16*)d_in[4];
  P.Cre = (const u16*)d_in[5];
  P.Cim = (const u16*)d_in[6];
  P.logdt = (const u16*)d_in[7];
  P.dskip = (const u16*)d_in[8];
  P.w1 = (const u16*)d_in[10];
  P.b1 = (const u16*)d_in[11];
  P.w2 = (const u16*)d_in[12];
  P.out = (u16*)d_out;
  P.ws = (char*)d_ws;
  (void)hipFuncSetAttribute((const void*)fused_kernel, hipFuncAttributeMaxDynamicSharedMemorySize, LDS_BYTES);
  fused_kernel<<<256, 512, LDS_BYTES, stream>>>(P);
}
```

```cpp
#if defined(__HIP_DEVICE_COMPILE__)
#pragma clang attribute push(__attribute__((target("no-packed-fp32-ops"))), apply_to = function)
#endif
#include <hip/hip_runtime.h>
#include <stdint.h>
#include <type_traits>

typedef unsigned short u16;
typedef unsigned int u32;
typedef unsigned long long u64;
typedef short s16x4 __attribute__((ext_vector_type(4)));
typedef _Float16 h16x2 __attribute__((ext_vector_type(2)));
typedef _Float16 h16x8 __attribute__((ext_vector_type(8)));
typedef __bf16 b16x2 __attribute__((ext_vector_type(2)));
typedef __bf16 b16x8 __attribute__((ext_vector_type(8)));
typedef float f32x2 __attribute__((ext_vector_type(2)));
typedef float f32x4 __attribute__((ext_vector_type(4)));
typedef u32 u32x2 __attribute__((ext_vector_type(2)));
typedef u32 u32x4 __attribute__((ext_vector_type(4)));
typedef int i32x4 __attribute__((ext_vector_type(4)));
#define LAS __attribute__((address_space(3)))
#define GAS __attribute__((address_space(1)))
#define RLX_AGENT __ATOMIC_RELAXED, __HIP_MEMORY_SCOPE_AGENT

constexpr int L_ = 2048, D_ = 512, N_ = 128, F_ = 1792;
#define CONV_SPLIT 0
#define S_PRIO 1
#define POLL_SLEEP 8
#define ROT 0
#define CODE_PF_BYTES 16384

constexpr u32 WS_STAT = 4096;
constexpr u32 FSTR = 128;
constexpr u32 WS_FXN = 32768;
constexpr u32 WS_FACT = WS_FXN + 256 * FSTR;
constexpr u32 WS_FY = WS_FACT + 256 * FSTR;
constexpr u32 WS_XNT = 131072;
constexpr u32 WS_ACT = WS_XNT + 2097152;
constexpr u32 WS_YT = WS_ACT + 7340032;
constexpr u32 WS_END = WS_YT + 2097152;
#define DBG_OFF (16u << 20)

constexpr u32 STAGE = 32768;
constexpr u32 KT = 131072;
constexpr u32 KTW = 4352;
constexpr u32 XT = 148480;
constexpr u32 KX = 148480;
constexpr u32 KRO = 148480;
constexpr u32 KRS = 4576;
constexpr u32 DUM = 157632;
constexpr u32 SYN = 158656;
constexpr u32 CSO = 158784;
constexpr u32 STO = 129024;
constexpr u32 B1O = 129536;
constexpr u32 KLIN = 161248;
constexpr u32 KLS = 1296;
constexpr u32 UC = 98304;
constexpr u32 UCS = 4160;
constexpr u32 CRED = 131584;
constexpr u32 YI = 131584;
constexpr u32 LDS_BYTES = 163840;
enum { C_LANDED = 0, E_XT = 16, E_XTR, E_XNST, E_KX, E_KLIN, E_UC, E_CONV, E_YST, E_CS, E_ACT, E_CRED, E_W2 };

constexpr int SPIN_CAP = 1 << 16;

__device__ __forceinline__ float bflo(u32 v) { return __uint_as_float(v << 16); }
__device__ __forceinline__ float bfhi(u32 v) { return __uint_as_float(v & 0xffff0000u); }
__device__ __forceinline__ u32 pkbf(float a, float b) {
  f32x2 v = {a, b};
  return __builtin_bit_cast(u32, __builtin_convertvector(v, b16x2));
}
__device__ __forceinline__ u32 pkh(float a, float b) {
  f32x2 v = {a, b};
  return __builtin_bit_cast(u32, __builtin_convertvector(v, h16x2));
}
__device__ __forceinline__ float rbf(float a) { return bflo(pkbf(a, 0.f)); }

__device__ __forceinline__ i32x4 mk_srd(const void* p, u32 bytes) {
  u64 a = (u64)p;
  i32x4 r;
  r[0] = (int)(u32)a;
  r[1] = (int)((u32)(a >> 32) & 0xffffu);
  r[2] = (int)bytes;
  r[3] = 0x00020000;
  return r;
}
__device__ __forceinline__ void dma16(i32x4 srd, u32 voff, u32 soff, u32 dst) {
  asm volatile("s_mov_b32 m0, %3\n\ts_nop 0\n\tbuffer_load_dwordx4 %0, %1, %2 offen lds"
               :: "v"(voff), "s"(srd), "s"(__builtin_amdgcn_readfirstlane(soff)), "s"(__builtin_amdgcn_readfirstlane(dst)));
}
__device__ __forceinline__ u32 ald4(i32x4 srd, u32 voff) {
  u32 r;
  asm volatile("buffer_load_dword %0, %1, %2, 0 offen" : "=v"(r) : "v"(voff), "s"(srd) : "memory");
  return r;
}
__device__ __forceinline__ u32x2 ald8(i32x4 srd, u32 voff) {
  u32x2 r;
  asm volatile("buffer_load_dwordx2 %0, %1, %2, 0 offen" : "=v"(r) : "v"(voff), "s"(srd) : "memory");
  return r;
}
__device__ __forceinline__ u32x4 ald16(i32x4 srd, u32 voff) {
  u32x4 r;
  asm volatile("buffer_load_dwordx4 %0, %1, %2, 0 offen" : "=v"(r) : "v"(voff), "s"(srd) : "memory");
  return r;
}
#define BAR() asm volatile("s_waitcnt lgkmcnt(0)\n\ts_barrier" ::: "memory")
#define WAITV(n) asm volatile("s_waitcnt vmcnt(" #n ")" ::: "memory")
#define SB() __builtin_amdgcn_sched_barrier(0)

__device__ __forceinline__ f32x4 mfma_bf(u32x4 a, u32x4 b, f32x4 c) {
  return __builtin_amdgcn_mfma_f32_16x16x32_bf16(__builtin_bit_cast(b16x8, a), __builtin_bit_cast(b16x8, b), c, 0, 0, 0);
}
__device__ __forceinline__ f32x4 mfma_h(u32x4 a, u32x4 b, f32x4 c) {
  return __builtin_amdgcn_mfma_f32_16x16x32_f16(__builtin_bit_cast(h16x8, a), __builtin_bit_cast(h16x8, b), c, 0, 0, 0);
}
__device__ __forceinline__ u32x2 tr_rd(LAS char* p) {
  return __builtin_bit_cast(u32x2, __builtin_amdgcn_ds_read_tr16_b64_v4i16((LAS s16x4*)p));
}
__device__ __forceinline__ u32x4 cat(u32x2 a, u32x2 b) { return u32x4{a[0], a[1], b[0], b[1]}; }
template <int CTRL> __device__ __forceinline__ float dpp_mov(float v) {
  return __builtin_bit_cast(float, __builtin_amdgcn_update_dpp(0, __builtin_bit_cast(int, v), CTRL, 0xf, 0xf, true));
}
__device__ __forceinline__ float rdlane(float v, int i) {
  return __builtin_bit_cast(float, __builtin_amdgcn_readlane(__builtin_bit_cast(int, v), i));
}
__device__ __forceinline__ float wave_max(float v) {
  v = fmaxf(v, dpp_mov<0xB1>(v));
  v = fmaxf(v, dpp_mov<0x4E>(v));
  v = fmaxf(v, dpp_mov<0x141>(v));
  v = fmaxf(v, dpp_mov<0x140>(v));
  return fmaxf(fmaxf(rdlane(v, 0), rdlane(v, 16)), fmaxf(rdlane(v, 32), rdlane(v, 48)));
}
__device__ __forceinline__ float wave_sum(float v) {
  v += dpp_mov<0xB1>(v);
  v += dpp_mov<0x4E>(v);
  v += dpp_mov<0x141>(v);
  v += dpp_mov<0x140>(v);
  return (rdlane(v, 0) + rdlane(v, 16)) + (rdlane(v, 32) + rdlane(v, 48));
}
__device__ __forceinline__ u64 probe(GAS u64* p) { return __hip_atomic_load(p, RLX_AGENT); }
__device__ __forceinline__ void wait_flags(GAS u64* p, u64 v, u64 tag) {
  if (!__all(v == tag)) {
#pragma unroll 1
    for (int it = 0; it < SPIN_CAP; ++it) {
      __builtin_amdgcn_s_sleep(POLL_SLEEP);
      v = __hip_atomic_load(p, RLX_AGENT);
      if (__all(v == tag)) break;
    }
  }
  __builtin_amdgcn_fence(__ATOMIC_ACQUIRE, "wavefront");
}
template <int I, int N, class F> __device__ __forceinline__ void static_for(F&& f) {
  if constexpr (I < N) { f(std::integral_constant<int, I>{}); static_for<I + 1, N>(f); }
}
#define CV(x) decltype(x)::value
__device__ __forceinline__ void lds_arrive(u32 addr) {
  u32 one = 1;
  asm volatile("s_mov_b64 exec, 1\n\tds_add_u32 %0, %1\n\ts_mov_b64 exec, -1" :: "v"(addr), "v"(one) : "memory");
}
__device__ __forceinline__ void lds_wait(LAS char* p, u32 target) {
#pragma unroll 1
  for (int it = 0; it < (1 << 22); ++it) {
    u32 v = *(volatile LAS u32*)p;
    if ((u32)__builtin_amdgcn_readfirstlane(v) >= target) break;
    __builtin_amdgcn_s_sleep(1);
  }
  asm volatile("" ::: "memory");
}
__device__ __forceinline__ void lds_spin(LAS char* p, u32 target) {
#pragma unroll 1
  for (int it = 0; it < (1 << 24); ++it) {
    u32 v = *(volatile LAS u32*)p;
    if ((u32)__builtin_amdgcn_readfirstlane(v) >= target) break;
  }
  asm volatile("" ::: "memory");
}
__device__ __forceinline__ LAS char* opq(LAS char* p) { asm volatile("" : "+v"(p)); return p; }
__device__ __forceinline__ float half_sum(float v) {
  v += dpp_mov<0xB1>(v);
  v += dpp_mov<0x4E>(v);
  v += dpp_mov<0x141>(v);
  v += dpp_mov<0x140>(v);
  auto r = __builtin_amdgcn_permlane16_swap(__builtin_bit_cast(u32, v), __builtin_bit_cast(u32, v), false, false);
  return __builtin_bit_cast(float, (u32)r[0]) + __builtin_bit_cast(float, (u32)r[1]);
}
struct cpx { float re, im; };
__device__ __forceinline__ cpx cmul(cpx a, cpx b) {
  cpx r;
  r.re = fmaf(a.re, b.re, -(a.im * b.im));
  r.im = fmaf(a.re, b.im, a.im * b.re);
  return r;
}

#define STAMP(k)

struct Params {
  const u16 *x, *Are, *Aim, *Bre, *Bim, *Cre, *Cim, *logdt, *dskip, *w1, *b1, *w2;
  u16* out;
  char* ws;
};

__global__ void __launch_bounds__(512, 2) fused_kernel(Params P) {
  extern __shared__ __attribute__((aligned(16))) char smem[];
  LAS char* S = (LAS char*)smem;
  const u32 sbase = (u32)(size_t)S;
  char* ws = P.ws;
  const int t = __builtin_amdgcn_workitem_id_x(), l = t & 63, w = __builtin_amdgcn_readfirstlane(t >> 6);
  const int h = l >> 4, l15 = l & 15;
  const int b = __builtin_amdgcn_workgroup_id_x(), xcd = b & 7, rk = b >> 3;
  const int bi = 4 * (xcd >> 1) + (rk >> 3), bj = 8 * (xcd & 1) + (rk & 7);
  const int bp = 4 * xcd + (rk >> 3), bc = rk & 7;
  const int q4 = w & 3;
  STAMP(0);

  const __amdgpu_buffer_rsrc_t rs_ws = __builtin_amdgcn_make_buffer_rsrc((void*)ws, 0, WS_END, 0x00020000);
  const __amdgpu_buffer_rsrc_t rs_x = __builtin_amdgcn_make_buffer_rsrc((void*)P.x, 0, L_ * D_ * 2, 0x00020000);
  const __amdgpu_buffer_rsrc_t rs_out = __builtin_amdgcn_make_buffer_rsrc((void*)P.out, 0, L_ * D_ * 2, 0x00020000);
  const i32x4 sx = mk_srd(P.x, L_ * D_ * 2), sw1 = mk_srd(P.w1, D_ * F_ * 2), sw2 = mk_srd(P.w2, D_ * F_ * 2);
  const u64 tag = *(const u64*)ws + 1ull;

  u32 p_ar, p_ai, p_br, p_bi, p_cr, p_ci;
  u32x4 sr0, sr1;
  const int cc = q4 >> 1, hm = q4 & 1, ch = 2 * b + cc;
  if (w < 4) {
    const u32 o = (128 * bi + 8 * bj + 2 * q4) * 1024 + l * 32;
    sr0 = ald16(sx, o);
    sr1 = ald16(sx, o + 16);
    if (t < 32) *(LAS u32*)(S + SYN + t * 4) = 0;
  } else {
    const u32 po = ch * 256 + 4 * l;
    p_ar = ald4(mk_srd(P.Are, D_ * N_ * 2), po);
    p_ai = ald4(mk_srd(P.Aim, D_ * N_ * 2), po);
    p_br = ald4(mk_srd(P.Bre, D_ * N_ * 2), po);
    p_bi = ald4(mk_srd(P.Bim, D_ * N_ * 2), po);
    p_cr = ald4(mk_srd(P.Cre, D_ * N_ * 2), po);
    p_ci = ald4(mk_srd(P.Cim, D_ * N_ * 2), po);
  }
  const u32 dq = q4 < 3 ? 4096u : 12288u, dq2 = q4 < 3 ? 11264u : 12288u;
  const u32 vx = (128 * bi + 8 * q4 + (l >> 3)) * 1024 + (((l & 7) ^ (l >> 3)) * 16);
  u32 vwa, vwb;
  {
    const u32 g0 = q4 * 64 + l, pos0 = (g0 * 4682u) >> 16, c0 = g0 - pos0 * 14;
    const u32 g1 = g0 + 256, pos1 = (g1 * 4682u) >> 16, c1 = g1 - pos1 * 14;
    const u32 k0 = (pos0 & ~12u) | ((pos0 & 4) << 1) | ((pos0 & 8) >> 1);
    const u32 k1 = (pos1 & ~12u) | ((pos1 & 4) << 1) | ((pos1 & 8) >> 1);
    vwa = k0 * (F_ * 2) + bj * 224 + c0 * 16;
    vwb = q4 < 3 ? k1 * (F_ * 2) + bj * 224 + c1 * 16 : 0x7ff00000u;
  }
  BAR();
  auto issue_stage = [&](int c) {
    const u32 sb_ = sbase + c * STAGE + q4 * 1024;
#pragma unroll
    for (int i = 0; i < 4; ++i) dma16(sx, vx, c * 128 + i * 32768, sb_ + i * 4096);
    const u32 so = c * (64 * F_ * 2);
    dma16(sw1, vwa, so, sb_ + 16384);
    dma16(sw1, vwa, so + 32 * F_ * 2, sb_ + 16384 + 7168);
    dma16(sw1, vwb, so, sb_ + 16384 + dq);
    dma16(sw1, vwb, so + 32 * F_ * 2, sb_ + 16384 + dq2);
  };
  issue_stage(w >> 2);
  issue_stage(2 + (w >> 2));
  u32 w2o;
  LAS char* w2a[4];
  {
    const int rr = l >> 3, slot = l & 7;
    const int dt = (slot >> 1) ^ ((rr >> 1) & 3);
    w2o = (224 * w + rr) * 1024 + bc * 128 + (2 * dt + (slot & 1)) * 16;
    const int q = l15 >> 2, p = l & 3, x = (2 * h + (q >> 1)) & 3;
#pragma unroll
    for (int d = 0; d < 4; ++d) w2a[d] = S + 4096 * w + (4 * h + q) * 128 + ((d ^ x) * 32) + (p >> 1) * 16 + (p & 1) * 8;
  }
#define W2TILE(ks, hf) ((ks) < 3 ? 2 * (ks) + (hf) : ((ks) == 3 ? ((hf) ? 13 : 6) : 7 + 2 * ((ks) - 4) + (hf)))
#define ISSUE_W2(p, n, slot) dma16(sw2, w2o, (W2TILE(p, (n) >> 1) * 16 + ((n) & 1) * 8) * 1024, sbase + (slot) * STAGE + 4096 * w + (n) * 1024)
  auto w2_frag = [&](int d, int slot) {
    LAS char* p = slot == 2 ? opq(w2a[d] + 2 * STAGE) : w2a[d] + slot * STAGE;
    return cat(tr_rd(p), tr_rd(p + 2048));
  };
  u32x4 wr1[4][4];
  auto w2_round2 = [&]() {
#pragma unroll
    for (int k = 0; k < 3; ++k)
#pragma unroll
      for (int d = 0; d < 4; ++d) wr1[k][d] = w2_frag(d, k);
    asm volatile("s_waitcnt lgkmcnt(0)" ::: "memory");
#pragma unroll
    for (int n = 0; n < 4; ++n) ISSUE_W2(3, n, 0);
#pragma unroll
    for (int n = 0; n < 4; ++n) ISSUE_W2(4, n, 1);
#pragma unroll
    for (int n = 0; n < 4; ++n) ISSUE_W2(5, n, 2);
  };
  f32x4 ca[4];
  auto conv_half = [&]() {
#pragma unroll
    for (int q = 0; q < 4; ++q) ca[q] = f32x4{0, 0, 0, 0};
    const int v = l15 + 1, sft = v & 3;
    LAS char* ubase = opq(S + UC + (cc * 4 + sft) * UCS + (8 * h + v - sft) * 2 + (w < 4 ? 256 : 0));
    LAS char* kbase = opq(S + KRO + cc * KRS + (2032 - 1792 - 16 * l15 + 8 * h) * 2 + (w < 4 ? 256 : 0));
    auto cv = [&](auto Gc) {
      constexpr int G = CV(Gc);
      constexpr int TA = G ? 1 : 0, TB = G ? 2 : 3, TC = G ? 5 : 4, TD = G ? 6 : 7;
      u32x4 U[2][TD + 1], Kf[2][TD + 1];
      auto ldu = [&](LAS char* p) { return cat(*(volatile LAS u32x2*)(p), *(volatile LAS u32x2*)(p + 8)); };
#pragma unroll
      for (int i = 0; i <= TD; ++i) { U[0][i] = ldu(ubase + i * 512); Kf[0][TD - i] = *(LAS u32x4*)(kbase + (7 - (TD - i)) * 512); }
#pragma unroll 1
      for (int rp = 0; rp < (CONV_SPLIT ? 2 : 4); ++rp) {
        static_for<0, 2>([&](auto pc) {
          constexpr int p = CV(pc);
          static_for<0, TD + 1>([&](auto ic) {
            constexpr int i = CV(ic);
            SB();
            ca[3] = mfma_h(U[p][i], Kf[p][TD - i], ca[3]);
            SB();
            U[p ^ 1][i] = ldu(ubase + (p + 1) * 64 + i * 512);
            SB();
            if constexpr (i >= TD - TC) ca[2] = mfma_h(U[p][i - (TD - TC)], Kf[p][TD - i], ca[2]);
            SB();
            Kf[p ^ 1][TD - i] = *(LAS u32x4*)(kbase + (p + 1) * 64 + (7 - (TD - i)) * 512);
            SB();
            if constexpr (i >= TD - TB) ca[1] = mfma_h(U[p][i - (TD - TB)], Kf[p][TD - i], ca[1]);
            if constexpr (i >= TD - TA) ca[0] = mfma_h(U[p][i - (TD - TA)], Kf[p][TD - i], ca[0]);
          });
        });
        SB();
        ubase = opq(ubase + 128); kbase = opq(kbase + 128);
      }
    };
    if (hm == 0) cv(std::integral_constant<int, 0>{}); else cv(std::integral_constant<int, 1>{});
  };
  const u32 syn = sbase + SYN;
  LAS char* synp = opq(S + SYN);
#define ARRIVE(e) lds_arrive(syn + (e) * 4)
#define AWAIT(e) lds_wait(synp + (e) * 4, 4)

  if (w < 4) {
    WAITV(16);
    asm volatile("" : "+v"(sr0), "+v"(sr1));
    {
      float e[16];
#pragma unroll
      for (int q = 0; q < 4; ++q) { e[2 * q] = bflo(sr0[q]); e[2 * q + 1] = bfhi(sr0[q]); e[8 + 2 * q] = bflo(sr1[q]); e[9 + 2 * q] = bfhi(sr1[q]); }
      float s0 = (e[0] + e[1]) + (e[2] + e[3]), s1 = (e[4] + e[5]) + (e[6] + e[7]), s2 = (e[8] + e[9]) + (e[10] + e[11]), s3 = (e[12] + e[13]) + (e[14] + e[15]);
      const float mean = half_sum((s0 + s1) + (s2 + s3)) * (1.0f / D_);
      float v0 = 0, v1 = 0;
#pragma unroll
      for (int q = 0; q < 8; ++q) { float d0 = e[q] - mean, d1 = e[8 + q] - mean; v0 = fmaf(d0, d0, v0); v1 = fmaf(d1, d1, v1); }
      const float var = half_sum(v0 + v1) * (1.0f / D_);
      const float sd = __builtin_amdgcn_sqrtf(rbf(rbf(var) + 1e-5f));
      const u32 pay = pkbf(mean, sd);
      if ((l & 31) == 0) __hip_atomic_store((GAS u64*)(ws + WS_STAT + (128 * bi + 8 * bj + 2 * q4 + (l >> 5)) * 8), ((u64)(u32)tag << 32) | pay, RLX_AGENT);
    }
    f32x4 acc[7][2], accs[7];
#pragma unroll
    for (int f = 0; f < 7; ++f) { acc[f][0] = f32x4{0, 0, 0, 0}; acc[f][1] = f32x4{0, 0, 0, 0}; accs[f] = f32x4{0, 0, 0, 0}; }
    u32 xa = (32 * q4 + l15) * 128 + ((h ^ (l & 7)) * 16);
    u32 wo = 16384 + (16 * (h >> 1) + 4 * (h & 1) + (l15 >> 2)) * 224 + (l & 3) * 8;
    u32x4 ones = {0x3f803f80u, 0x3f803f80u, 0x3f803f80u, 0x3f803f80u};
    asm volatile("" : "+v"(ones));
    u32x4 xf[2][2];
    u32x2 wl[2][7], wh[2][7];
    u32 lnd = 0;
    auto kstep = [&](auto kc, auto mc, u32 so, u32 dst, u32 cnt_addr, LAS char* cnt_p, u32 tgt, bool dowait) {
      constexpr int k = CV(kc), n = k ^ 1, MODE = CV(mc);
      constexpr int r0 = k ? 4 : 0;
      LAS char* xp = S + (k ? xa : (xa ^ 64));
      LAS char* wp = S + wo + (k ? 0 : 32 * 224);
      static_for<0, 14>([&](auto jc) {
        constexpr int j = CV(jc), f = j >> 1, mt = j & 1;
        SB();
        acc[f][mt] = mfma_bf(cat(wl[k][f], wh[k][f]), xf[k][mt], acc[f][mt]);
        SB();
        if constexpr (k == 1 && j == 1) lnd = *(volatile LAS u32*)cnt_p;
        if constexpr (k == 1 && j == 3) { if ((u32)__builtin_amdgcn_readfirstlane(lnd) < tgt) lds_spin(cnt_p, tgt); }
        if constexpr (j == r0) { xf[n][0] = *(LAS u32x4*)(xp); xf[n][1] = *(LAS u32x4*)(xp + 2048); }
        if constexpr (j >= r0 + 1 && j <= r0 + 7) {
          wl[n][j - r0 - 1] = tr_rd(wp + (j - r0 - 1) * 32);
          wh[n][j - r0 - 1] = tr_rd(wp + 8 * 224 + (j - r0 - 1) * 32);
        }
        constexpr int pc = j == (k ? 5 : 1) ? 0 : j == (k ? 8 : 5) ? 1 : j == (k ? 11 : 10) ? 2 : j == (k ? 13 : 12) ? 3 : -1;
        if constexpr (pc >= 0 && MODE == 1 && k == 0)
          dma16(sw1, pc < 2 ? vwa : vwb, so + (pc & 1) * (32 * F_ * 2), dst + (pc == 0 ? 0u : pc == 1 ? 7168u : pc == 2 ? dq : dq2));
        if constexpr (pc >= 0 && MODE == 1 && k == 1) dma16(sx, vx, so + pc * 32768, dst + pc * 4096);
        if constexpr (pc >= 0 && MODE == 2) dma16(sw2, w2o, so + pc * 8192, dst + pc * 1024);
        if constexpr (k == 0 && j == 8) {
          if (dowait) WAITV(14);
          lds_arrive(cnt_addr);
        }
      });
      SB();
    };
    auto colsum = [&](auto kc) {
#pragma unroll
      for (int f = 0; f < 7; ++f) accs[f] = mfma_bf(cat(wl[CV(kc)][f], wh[CV(kc)][f]), ones, accs[f]);
    };
    using I0 = std::integral_constant<int, 0>; using I1 = std::integral_constant<int, 1>; using I2 = std::integral_constant<int, 2>;
    WAITV(9);
    ARRIVE(C_LANDED);
    lds_spin(synp + C_LANDED * 4, 4);
    xf[0][0] = *(LAS u32x4*)(S + xa); xf[0][1] = *(LAS u32x4*)(S + xa + 2048);
#pragma unroll
    for (int f = 0; f < 7; ++f) { wl[0][f] = tr_rd(S + wo + f * 32); wh[0][f] = tr_rd(S + wo + 8 * 224 + f * 32); }
    auto adv = [&]() { xa = (xa + STAGE) & 131071u; wo = (wo + STAGE) & 131071u; };
    kstep(I0{}, I0{}, 0, 0, syn + 4, synp, 0, false);
    if (q4 == 0) colsum(I0{});
    adv();
    kstep(I1{}, I1{}, 4 * 128, sbase + q4 * 1024, 0, synp + 4, 8, false);
    if (q4 == 1) colsum(I1{});
    WAITV(4);
#pragma unroll 1
    for (int c = 1; c < 4; ++c) {
      kstep(I0{}, I1{}, (c + 3) * (64 * F_ * 2), sbase + (c - 1) * STAGE + 16384 + q4 * 1024, syn + (c + 1) * 4, synp, 0, true);
      if (((2 * c) & 3) == q4) colsum(I0{});
      adv();
      kstep(I1{}, I1{}, (c + 4) * 128, sbase + c * STAGE + q4 * 1024, 0, synp + (c + 1) * 4, c == 2 ? 8u : 4u, false);
      if (((2 * c + 1) & 3) == q4) colsum(I1{});
    }
    kstep(I0{}, I1{}, 7 * (64 * F_ * 2), sbase + 3 * STAGE + 16384 + q4 * 1024, syn + 5 * 4, synp, 0, true);
    if (q4 == 0) colsum(I0{});
#pragma unroll 1
    for (int p = 0; p < 3; ++p) {
      adv();
      kstep(I1{}, I2{}, p * 32768, sbase + p * STAGE + 4096 * w, 0, synp + (5 + p) * 4, 4, false);
      if (((2 * p + 1) & 3) == q4) colsum(I1{});
      kstep(I0{}, I2{}, p * 32768 + 896 * 1024, sbase + p * STAGE + 4096 * (w + 4), syn + (6 + p) * 4, synp, 0, p < 2);
      if (((2 * p + 2) & 3) == q4) colsum(I0{});
    }
    kstep(I1{}, I0{}, 0, 0, 0, synp + 8 * 4, 0, false);
    if (q4 == 3) colsum(I1{});
    if (l15 == 0) {
      LAS char* cp = opq(S + CSO + q4 * 448 + h * 16);
#pragma unroll
      for (int f = 0; f < 7; ++f) *(LAS f32x4*)(cp + 64 * f) = accs[f];
    }
    AWAIT(E_XT);
    u32 pay2[2];
    u32x2 b1v[7];
    {
      LAS char* sp = opq(S + STO + (32 * q4 + l15) * 4);
      LAS char* bp_ = opq(S + B1O + h * 8);
      pay2[0] = *(LAS u32*)sp; pay2[1] = *(LAS u32*)(sp + 64);
#pragma unroll
      for (int f = 0; f < 7; ++f) b1v[f] = *(LAS u32x2*)(bp_ + 32 * f);
    }
    ARRIVE(E_CS);
    const float LOG2E = 1.4426950408889634f, LN2 = 0.6931471805599453f;
    float rs[2], tm[2];
#pragma unroll
    for (int mt = 0; mt < 2; ++mt) {
      rs[mt] = __builtin_amdgcn_rcpf(bfhi(pay2[mt])) * LOG2E;
      tm[mt] = -bflo(pay2[mt]) * rs[mt];
    }
    lds_spin(synp + E_CS * 4, 4);
    const int jp = bj & 1;
    const u32 abase = WS_ACT + ((8 * bi + 2 * q4) * 56 + 7 * (bj >> 1) + (jp ? 4 : 0)) * 1024 + l * 16;
    LAS char* csp = opq(S + CSO + h * 16);
    u32x2 pk[2][2];
    static_for<0, 7>([&](auto fc) {
      constexpr int f = CV(fc);
      const f32x4 cs = (*(LAS f32x4*)(csp + 64 * f) + *(LAS f32x4*)(csp + 64 * f + 448)) + (*(LAS f32x4*)(csp + 64 * f + 896) + *(LAS f32x4*)(csp + 64 * f + 1344));
      const float bia[4] = {bflo(b1v[f][0]) * LOG2E, bfhi(b1v[f][0]) * LOG2E, bflo(b1v[f][1]) * LOG2E, bfhi(b1v[f][1]) * LOG2E};
#pragma unroll
      for (int mt = 0; mt < 2; ++mt) {
        float a[4];
#pragma unroll
        for (int r = 0; r < 4; ++r) {
          float g = fmaf(acc[f][mt][r], rs[mt], fmaf(tm[mt], cs[r], bia[r]));
          float s1 = __builtin_amdgcn_exp2f(g) + 1.f;
          float rr = __builtin_amdgcn_rcpf(fmaf(s1, s1, 1.f));
          a[r] = g * fmaf(rr, -2.f * LN2, LN2);
        }
        pk[f & 1][mt] = u32x2{pkbf(a[0], a[1]), pkbf(a[2], a[3])};
      }
      if constexpr (f & 1) {
#pragma unroll
        for (int mt = 0; mt < 2; ++mt)
          __builtin_amdgcn_raw_buffer_store_b128(cat(pk[0][mt], pk[1][mt]), rs_ws, abase + (f >> 1) * 1024, mt * 56 * 1024, 16);
      }
      if constexpr (f == 6) {
#pragma unroll
        for (int mt = 0; mt < 2; ++mt)
          __builtin_amdgcn_raw_buffer_store_b64(pk[0][mt], rs_ws, abase + (jp ? -1024 + 8 : 3072), mt * 56 * 1024, 16);
      }
    });
    WAITV(8);
    ARRIVE(E_W2);
    w2_round2();
    WAITV(12);
    ARRIVE(E_ACT);
    if (w == 0) {
      lds_spin(synp + E_ACT * 4, 4);
      if (l == 0) __hip_atomic_store((GAS u64*)(ws + WS_FACT + (bi * 16 + bj) * FSTR), tag, RLX_AGENT);
    }
  } else {
    const int ts = t - 256;
    __builtin_amdgcn_s_setprio(S_PRIO);
    const u32 ldt2 = *(const u32*)(P.logdt + 2 * b);
    const u32 dsk2 = *(const u32*)(P.dskip + 2 * b);
    u32x4 xs0 = ald16(sx, (128 * bi + (ts >> 1)) * 1024 + bj * 64 + (ts & 1) * 32);
    u32x4 xs1 = ald16(sx, (128 * bi + (ts >> 1)) * 1024 + bj * 64 + (ts & 1) * 32 + 16);
    u32 b1w = ald4(mk_srd(P.b1, F_ * 2), bj * 224 + 4 * l);
    dma16(mk_srd((const void*)__builtin_amdgcn_s_getpc(), CODE_PF_BYTES), ((rk & 7) * 4 + q4) * 1024 + l * 16, 0, sbase + DUM);
    WAITV(12);
    asm volatile("" : "+v"(p_ar), "+v"(p_ai), "+v"(p_br), "+v"(p_bi), "+v"(p_cr), "+v"(p_ci));
    ARRIVE(C_LANDED + 1);
    float yscale, gmax, zr, zi;
    cpx cbn;
    {
      const float LOG2E = 1.4426950408889634f, INV2PI = 0.15915494309189535f;
      const float lx = cc ? bfhi(ldt2) : bflo(ldt2);
      const float th = lx * LOG2E, tl = fmaf(lx, LOG2E, -th) + lx * 1.925963033500011e-8f;
      const float dt = rbf(__builtin_amdgcn_exp2f(th) * fmaf(tl, 0.6931471805599453f, 1.0f));
      cpx cb0 = cmul(cpx{bflo(p_cr), bflo(p_ci)}, cpx{dt * bflo(p_br), dt * bflo(p_bi)});
      cpx cb1 = cmul(cpx{bfhi(p_cr), bfhi(p_ci)}, cpx{dt * bfhi(p_br), dt * bfhi(p_bi)});
      const float z0 = dt * bflo(p_ar) * LOG2E, z1 = dt * bfhi(p_ar) * LOG2E;
      gmax = fmaxf(wave_max(fmaxf(z0, z1)), 0.f);
      const float cbmax = wave_max(fmaxf(fmaxf(fabsf(cb0.re), fabsf(cb0.im)), fmaxf(fabsf(cb1.re), fabsf(cb1.im))));
      const float cbs = __uint_as_float((__float_as_uint(cbmax) + 0x007fffffu) & 0x7f800000u);
      const float icb = cbs > 0.f ? __builtin_amdgcn_rcpf(cbs) : 0.f;
      yscale = cbs * __builtin_amdgcn_exp2f(gmax * 2048.f) * (1.0f / 64.f);
      zr = fmaxf((hm ? z1 : z0) - gmax, -1e30f);
      zi = dt * (hm ? bfhi(p_ai) : bflo(p_ai)) * INV2PI;
      cbn = hm ? cb1 : cb0;
      cbn.re *= 64.f * icb; cbn.im *= -64.f * icb;
    }
    auto cexp = [&](float a) {
      float mg = __builtin_amdgcn_exp2f(zr * a);
      float ph = __builtin_amdgcn_fractf(zi * a);
      return cpx{mg * __builtin_amdgcn_cosf(ph), mg * __builtin_amdgcn_sinf(ph)};
    };
    const cpx e1 = cexp(1.f), e8 = cexp(8.f), e128 = cexp(128.f);
    SB();
    WAITV(4);
    ARRIVE(C_LANDED + 3);
    GAS u64* stat_p = (GAS u64*)(ws + WS_STAT + (128 * bi + (ts >> 1)) * 8);
    u64 stat_v = probe(stat_p);
    LAS char* tb = opq(S + KT + q4 * KTW + l * 4);
    LAS char* tf = opq(S + KT + q4 * KTW + l15 * 272 + h * 16);
    auto put = [&](int row, cpx v) { *(volatile LAS u32*)(tb + row * 272) = pkh(v.re, v.im); };
    u32x4 fa[4];
    f32x4 kacc[3];
    auto tile_mma = [&]() {
      f32x4 a = {0, 0, 0, 0};
#pragma unroll
      for (int k = 0; k < 4; ++k) a = mfma_h(fa[k], *(volatile LAS u32x4*)(tf + k * 64), a);
      return a;
    };
    {
      cpx wv = cbn;
      const cpx e8c = cpx{e8.re, -e8.im};
#pragma unroll
      for (int q = 0; q < 16; ++q) { put(q, wv); if (q < 15) wv = cmul(wv, e8c); }
#pragma unroll
      for (int k = 0; k < 4; ++k) fa[k] = *(volatile LAS u32x4*)(tf + k * 64);
    }
    if (!__all((u32)(stat_v >> 32) == (u32)tag)) {
#pragma unroll 1
      for (int it = 0; it < SPIN_CAP; ++it) {
        __builtin_amdgcn_s_sleep(2);
        stat_v = probe(stat_p);
        if (__all((u32)(stat_v >> 32) == (u32)tag)) break;
      }
    }
    WAITV(0);
    asm volatile("" : "+v"(xs0), "+v"(xs1), "+v"(b1w));
    {
      const u32 pay = (u32)stat_v;
      const float mb = bflo(pay), rsd = __builtin_amdgcn_rcpf(bfhi(pay));
      *(LAS u32*)(S + STO + (ts >> 1) * 4) = pay;
      *(LAS u32*)(S + B1O + l * 4) = b1w;
      LAS char* xt = opq(S + XT + (ts & 1) * 16 * 272 + (ts >> 1) * 2);
#pragma unroll
      for (int q = 0; q < 8; ++q) {
        const u32 xv = q < 4 ? xs0[q] : xs1[q - 4];
        u32 dd = pkbf(bflo(xv) - mb, bfhi(xv) - mb);
        u32 xn = pkbf(bflo(dd) * rsd, bfhi(dd) * rsd);
        *(LAS u16*)(xt + (2 * q) * 272) = (u16)xn;
        *(LAS u16*)(xt + (2 * q + 1) * 272) = (u16)(xn >> 16);
      }
      ARRIVE(E_XT);
    }
    cpx e2, e4, e6;
    {
      cpx lo = cpx{1.f, 0.f}, g = e128;
#pragma unroll
      for (int q = 0; q < 8; ++q) {
        put(q, lo); put(8 + q, g);
        if (q == 2) e2 = lo;
        if (q == 4) e4 = lo;
        if (q == 6) e6 = lo;
        if (q < 7) { lo = (q == 0) ? e1 : cmul(lo, e1); g = cmul(g, e1); }
        if (q == 1) {
          AWAIT(E_XT);
          LAS char* xr = opq(S + XT + (ts >> 4) * 272 + (ts & 15) * 16);
          u32x4 v0 = *(LAS u32x4*)(xr), v1 = *(LAS u32x4*)(xr + 16 * 272);
          const u32 xo = WS_XNT + ((32 * bj + (ts >> 4)) * 2048 + 128 * bi + (ts & 15) * 8) * 2;
          __builtin_amdgcn_raw_buffer_store_b128(v0, rs_ws, xo, 0, 16);
          __builtin_amdgcn_raw_buffer_store_b128(v1, rs_ws, xo, 16 * 4096, 16);
          ARRIVE(E_XTR);
        }
      }
      kacc[0] = tile_mma();
    }
    cpx hv = cmul(e128, e128);
#pragma unroll
    for (int a = 2; a < 8; ++a) {
      if (a < 4) {
        put((a - 2) * 4 + 0, hv); put((a - 2) * 4 + 1, cmul(hv, e2));
        put((a - 2) * 4 + 2, cmul(hv, e4)); put((a - 2) * 4 + 3, cmul(hv, e6));
      } else {
        put(8 + (a - 4) * 2 + 0, hv); put(8 + (a - 4) * 2 + 1, cmul(hv, e4));
      }
      hv = cmul(hv, e128);
      if (a == 3) { WAITV(0); ARRIVE(E_XNST); }
    }
    kacc[1] = tile_mma();
    if (w == 4) {
      AWAIT(E_XNST);
      if (l == 0) __hip_atomic_store((GAS u64*)(ws + WS_FXN + (bj * 16 + bi) * FSTR), tag, RLX_AGENT);
    }
#pragma unroll
    for (int a = 8; a <= 16; ++a) { put(a - 8, hv); if (a < 16) hv = cmul(hv, e128); }
    kacc[2] = tile_mma();
    AWAIT(E_XTR);
    {
      LAS char* kx = opq(S + KX + q4 * 2048 + l * 16);
      *(LAS f32x4*)(kx) = hm ? kacc[0] : kacc[1];
      *(LAS f32x4*)(kx + 1024) = kacc[2];
      ARRIVE(E_KX);
      const int e = hm ? 1 + (l15 >> 3) : 0, lg = 3 - e, r8 = hm ? (l15 & 7) : l15;
      const int idx = (e ? 128 + 128 * e : 0) + ((r8 >> lg) << (7 - e)) + (r8 & ((1 << lg) - 1)) + ((4 * h) << lg);
      const int id2 = min(512 + 16 * l15 + 4 * h + 2 * hm, 642);
      LAS char* kl = S + KLIN + cc * KLS;
      AWAIT(E_KX);
      LAS char* ko_ = opq(S + KX + (q4 ^ 1) * 2048 + l * 16);
      f32x4 ka = (hm ? kacc[1] : kacc[0]) + *(LAS f32x4*)(ko_);
      f32x4 kb = kacc[2] + *(LAS f32x4*)(ko_ + 1024);
      float k2a = hm ? kb[2] : kb[0], k2b = hm ? kb[3] : kb[1];
      if (gmax > 0.f) {
#pragma unroll
        for (int r = 0; r < 4; ++r) ka[r] *= __builtin_amdgcn_exp2f(gmax * (float)(((idx + (r << lg) - 128 * e) << e) - 2048));
        k2a *= __builtin_amdgcn_exp2f(gmax * (float)(((id2 - 384) << 3) - 2048));
        k2b *= __builtin_amdgcn_exp2f(gmax * (float)(((id2 - 383) << 3) - 2048));
      }
#pragma unroll
      for (int r = 0; r < 4; ++r) *(LAS _Float16*)(kl + (idx + (r << lg)) * 2) = (_Float16)ka[r];
      *(LAS u32*)(kl + id2 * 2) = pkh(k2a, k2b);
    }
    ARRIVE(E_KLIN);
    GAS u64* fxn_p = (GAS u64*)(ws + WS_FXN + ((ch >> 5) * 16 + l15) * FSTR);
    u64 fxn_v = probe(fxn_p);
    const int tc = hm * 64 + l;
    AWAIT(E_KLIN);
    {
      LAS char* kl = S + KLIN + cc * KLS;
      const float jf = (float)(2047 - 16 * tc);
      const bool low = tc >= 120;
      u32 ko[8];
#pragma unroll
      for (int q = 0; q < 8; ++q) {
        const u32 bits = pkbf(jf - (float)(2 * q), jf - (float)(2 * q + 1));
        const u32 j0 = 2047 - 16 * tc - 2 * q;
        const u32 pr = low ? (j0 | ((j0 - 1) << 16)) : bits - 0x42804280u;
        ko[q] = (u32)*(LAS u16*)(kl + (pr & 0xffff) * 2) | ((u32)*(LAS u16*)(kl + (pr >> 16) * 2) << 16);
      }
      LAS char* kr = opq(S + KRO + cc * KRS + tc * 32);
      *(LAS u32x4*)(kr) = u32x4{ko[0], ko[1], ko[2], ko[3]};
      *(LAS u32x4*)(kr + 16) = u32x4{ko[4], ko[5], ko[6], ko[7]};
      *(LAS u32x4*)(S + KRO + cc * KRS + 4096 + min(tc, 29) * 16) = u32x4{0, 0, 0, 0};
    }
    wait_flags(fxn_p, fxn_v, tag);
    u32x2 us[4];
    const u32 yo = (ch * 2048 + 16 * l15 + 4 * h) * 2;
    const u32 t0 = hm ? 512u : 0u, t1 = hm ? 1024u : 1536u, t2 = hm ? 2560u : 2048u, t3 = hm ? 3072u : 3584u;
    {
      const u32 uo = WS_XNT + (ch * 2048 + tc * 16) * 2;
      u32x4 ua = __builtin_amdgcn_raw_buffer_load_b128(rs_ws, uo, 0, 16);
      u32x4 ub = __builtin_amdgcn_raw_buffer_load_b128(rs_ws, uo + 16, 0, 16);
      u32x2 uc = __builtin_amdgcn_raw_buffer_load_b64(rs_ws, uo + 32, 0, 16);
      us[0] = __builtin_amdgcn_raw_buffer_load_b64(rs_ws, yo, WS_XNT + t0, 16);
      us[1] = __builtin_amdgcn_raw_buffer_load_b64(rs_ws, yo, WS_XNT + t1, 16);
      us[2] = __builtin_amdgcn_raw_buffer_load_b64(rs_ws, yo, WS_XNT + t2, 16);
      us[3] = __builtin_amdgcn_raw_buffer_load_b64(rs_ws, yo, WS_XNT + t3, 16);
      u32 d[10], o[9];
#pragma unroll
      for (int q = 0; q < 4; ++q) { d[q] = pkh(bflo(ua[q]), bfhi(ua[q])); d[4 + q] = pkh(bflo(ub[q]), bfhi(ub[q])); }
#pragma unroll
      for (int q = 0; q < 2; ++q) d[8 + q] = pkh(bflo(uc[q]), bfhi(uc[q]));
#pragma unroll
      for (int q = 0; q < 9; ++q) o[q] = __builtin_amdgcn_alignbit(d[q + 1], d[q], 16);
      AWAIT(E_CS);
      LAS char* ucb = opq(S + UC + cc * 4 * UCS + (2 * tc + 2) * 16);
#pragma unroll
      for (int k = 0; k < 2; ++k) {
        *(LAS u32x4*)(ucb + k * 16) = u32x4{d[4 * k], d[4 * k + 1], d[4 * k + 2], d[4 * k + 3]};
        *(LAS u32x4*)(ucb + k * 16 + UCS) = u32x4{o[4 * k], o[4 * k + 1], o[4 * k + 2], o[4 * k + 3]};
        *(LAS u32x4*)(ucb + k * 16 + 2 * UCS) = u32x4{d[4 * k + 1], d[4 * k + 2], d[4 * k + 3], d[4 * k + 4]};
        *(LAS u32x4*)(ucb + k * 16 + 3 * UCS) = u32x4{o[4 * k + 1], o[4 * k + 2], o[4 * k + 3], o[4 * k + 4]};
      }
      if (tc == 0) {
        LAS char* z = ucb - 32;
#pragma unroll
        for (int s = 0; s < 4; ++s) *(LAS u32x4*)(z + s * UCS) = u32x4{0, 0, 0, 0};
        *(LAS u32x4*)(z + 16) = u32x4{0, 0, 0, 0};
        *(LAS u32x4*)(z + UCS + 16) = u32x4{0, 0, 0, d[0] << 16};
        *(LAS u32x4*)(z + 2 * UCS + 16) = u32x4{0, 0, 0, d[0]};
        *(LAS u32x4*)(z + 3 * UCS + 16) = u32x4{0, 0, d[0] << 16, o[0]};
      }
    }
    ARRIVE(E_UC);
    AWAIT(E_UC);
    conv_half();
    const float dsk = cc ? bfhi(dsk2) : bflo(dsk2);
    u32 tsk[4][2];
#pragma unroll
    for (int q = 0; q < 4; ++q) {
      tsk[q][0] = pkbf(bflo(us[q][0]) * dsk, bfhi(us[q][0]) * dsk);
      tsk[q][1] = pkbf(bflo(us[q][1]) * dsk, bfhi(us[q][1]) * dsk);
    }
    ARRIVE(E_CONV);
#pragma unroll
    for (int q = 0; q < 4; ++q) {
      const f32x4 a = ca[q];
      u32 y01 = pkbf(a[0] * yscale, a[1] * yscale), y23 = pkbf(a[2] * yscale, a[3] * yscale);
      u32x2 o = {pkbf(bflo(y01) + bflo(tsk[q][0]), bfhi(y01) + bfhi(tsk[q][0])), pkbf(bflo(y23) + bflo(tsk[q][1]), bfhi(y23) + bfhi(tsk[q][1]))};
      __builtin_amdgcn_raw_buffer_store_b64(o, rs_ws, yo, WS_YT + (q == 0 ? t0 : q == 1 ? t1 : q == 2 ? t2 : t3), 16);
    }
    AWAIT(E_W2);
    w2_round2();
    __builtin_amdgcn_s_setprio(0);
  }

  {
    GAS u64* fact_p = (GAS u64*)(ws + WS_FACT + ((bp >> 1) * 16 + 2 * w + (l & 1)) * FSTR);
    const u64 fact_v = probe(fact_p);
    WAITV(0);
    if (w >= 4) {
      ARRIVE(E_YST);
      if (w == 4) {
        lds_spin(synp + E_YST * 4, 4);
        if (l == 0) __hip_atomic_store((GAS u64*)(ws + WS_FY + b * FSTR), tag, RLX_AGENT);
      }
    }
    wait_flags(fact_p, fact_v, tag);
    const u32 ao = WS_ACT + (4 * bp * 56 + 7 * w) * 1024 + l * 16;
    u32x4 af[7][4];
    auto lda = [&](auto kc, auto lc) {
      af[CV(kc)][CV(lc)] = __builtin_amdgcn_raw_buffer_load_b128(rs_ws, ao, (CV(lc) * 56 + CV(kc)) * 1024, 16);
    };
    static_for<0, 3>([&](auto kc) { static_for<0, 4>([&](auto lc) { lda(kc, lc); }); });
#pragma unroll
    for (int d = 0; d < 4; ++d) wr1[3][d] = w2_frag(d, 0);
    asm volatile("s_waitcnt lgkmcnt(0)" ::: "memory");
#pragma unroll
    for (int n = 0; n < 4; ++n) ISSUE_W2(6, n, 0);
    f32x4 ac[4][4];
#pragma unroll
    for (int dt = 0; dt < 4; ++dt)
#pragma unroll
      for (int lt = 0; lt < 4; ++lt) ac[dt][lt] = f32x4{0, 0, 0, 0};
    GAS u64* fy_p = (GAS u64*)(ws + WS_FY + (32 * bc + 8 * q4 + (l & 7) + 0 * (l & 31)) * FSTR);
    u64 fy_v = 0;
    u32x4 xres;
    static_for<0, 7>([&](auto kc) {
      constexpr int ks = CV(kc);
      u32x4 wf[4];
#pragma unroll
      for (int dt = 0; dt < 4; ++dt) {
        if constexpr (ks < 4) wf[dt] = wr1[ks][dt];
        else wf[dt] = w2_frag(dt, ks == 6 ? 0 : ks - 3);
      }
      static_for<0, 4>([&](auto lc) {
        constexpr int lt = CV(lc);
        SB();
#pragma unroll
        for (int dt = 0; dt < 4; ++dt) ac[dt][lt] = mfma_bf(wf[dt], af[ks][lt], ac[dt][lt]);
        SB();
        if constexpr (ks + 3 < 7) lda(std::integral_constant<int, ks + 3>{}, lc);
        if constexpr (ks == 4 && lt == 0) {
          xres = __builtin_amdgcn_raw_buffer_load_b128(rs_x, (64 * bp + (t >> 3)) * 1024 + bc * 128 + (t & 7) * 16, 0, 0);
          if (w >= 4) fy_v = probe(fy_p);
        }
      });
    });
    AWAIT(E_CONV);
#pragma unroll
    for (int dt = 0; dt < 4; ++dt) {
      const int sg = ((2 * dt + (h >> 1)) & 3) * 4;
      LAS char* pw = opq(S + dt * STAGE + 4096 * w + h * 256 + ((l15 ^ sg) * 16));
#pragma unroll
      for (int lt = 0; lt < 4; ++lt) *(LAS f32x4*)(pw + lt * 1024) = ac[dt][lt];
    }
    if (w >= 4) {
      wait_flags(fy_p, fy_v, tag);
      const int yc = 16 * q4 + (l >> 3);
      const u32 yoff = WS_YT + ((64 * bc + yc) * 2048 + 64 * bp + (l & 7) * 8) * 2;
      u32x4 y0 = __builtin_amdgcn_raw_buffer_load_b128(rs_ws, yoff, 0, 16);
      u32x4 y1 = __builtin_amdgcn_raw_buffer_load_b128(rs_ws, yoff, 8 * 4096, 16);
      LAS char* yw = opq(S + YI + yc * 128 + (((l & 7) ^ (2 * q4)) * 16));
      *(LAS u32x4*)(yw) = y0;
      *(LAS u32x4*)((LAS char*)((size_t)yw ^ 16) + 1024) = y1;
    }
    BAR();
    {
      const int lrow = t >> 3, g = t & 7;
      LAS char* pr = opq(S + (g >> 1) * STAGE + (lrow >> 4) * 1024 + (2 * (g & 1)) * 256 + (((lrow & 15) ^ ((g & 3) * 4)) * 16));
      LAS char* yr = opq(S + YI + 8 * g * 128 + (((lrow >> 3) ^ g) * 16) + (lrow & 7) * 2);
      f32x4 s0 = *(LAS f32x4*)(pr), s1 = *(LAS f32x4*)(pr + 256);
#pragma unroll
      for (int p = 1; p < 8; ++p) {
        s0 += *(LAS f32x4*)(pr + 4096 * p);
        s1 += *(LAS f32x4*)(pr + 4096 * p + 256);
      }
      float yb[8] = {s0[0], s0[1], s0[2], s0[3], s1[0], s1[1], s1[2], s1[3]};
      u32 o[4];
#pragma unroll
      for (int q = 0; q < 4; ++q) {
        u32 y0 = *(LAS u16*)(yr + (2 * q) * 128);
        u32 y1 = *(LAS u16*)(yr + (2 * q + 1) * 128);
        u32 a = pkbf(bflo(xres[q]) + bflo(y0), bfhi(xres[q]) + bflo(y1));
        u32 bq = pkbf(yb[2 * q], yb[2 * q + 1]);
        o[q] = pkbf(bflo(a) + bflo(bq), bfhi(a) + bfhi(bq));
      }
      __builtin_amdgcn_raw_buffer_store_b128(u32x4{o[0], o[1], o[2], o[3]}, rs_out, (64 * bp + lrow) * 1024 + bc * 128 + g * 16, 0, 0);
    }
  }
  if (b == 0 && w == 0) {
#pragma unroll 1
    for (int q = 0; q < 4; ++q) {
      GAS u64* p = (GAS u64*)(ws + WS_FXN + (q * 64 + l) * FSTR);
      wait_flags(p, probe(p), tag);
    }
    if (l == 0) __hip_atomic_store((GAS u64*)ws, tag, RLX_AGENT);
  }
  STAMP(11);
}

#if defined(__HIP_DEVICE_COMPILE__)
#pragma clang attribute pop
#endif

extern "C" void kernel_launch(void* const* d_in, const int* in_sizes, int n_in, void* d_out, int out_size, void* d_ws,
                              size_t ws_size, hipStream_t stream) {
  Params P;
  P.x = (const u16*)d_in[0];
  P.Are = (const u16*)d_in[1];
  P.Aim = (const u16*)d_in[2];
  P.Bre = (const u16*)d_in[3];
  P.Bim = (const u16*)d_in[4];
  P.Cre = (const u16*)d_in[5];
  P.Cim = (const u16*)d_in[6];
  P.logdt = (const u16*)d_in[7];
  P.dskip = (const u16*)d_in[8];
  P.w1 = (const u16*)d_in[10];
  P.b1 = (const u16*)d_in[11];
  P.w2 = (const u16*)d_in[12];
  P.out = (u16*)d_out;
  P.ws = (char*)d_ws;
  (void)hipFuncSetAttribute((const void*)fused_kernel, hipFuncAttributeMaxDynamicSharedMemorySize, LDS_BYTES);
  fused_kernel<<<256, 512, LDS_BYTES, stream>>>(P);
}
```

```cpp
#if defined(__HIP_DEVICE_COMPILE__)
#pragma clang attribute push(__attribute__((target("no-packed-fp32-ops"))), apply_to = function)
#endif
#include <hip/hip_runtime.h>
#include <stdint.h>
#include <type_traits>

typedef unsigned short u16;
typedef unsigned int u32;
typedef unsigned long long u64;
typedef short s16x4 __attribute__((ext_vector_type(4)));
typedef _Float16 h16x2 __attribute__((ext_vector_type(2)));
typedef _Float16 h16x8 __attribute__((ext_vector_type(8)));
typedef __bf16 b16x2 __attribute__((ext_vector_type(2)));
typedef __bf16 b16x8 __attribute__((ext_vector_type(8)));
typedef float f32x2 __attribute__((ext_vector_type(2)));
typedef float f32x4 __attribute__((ext_vector_type(4)));
typedef u32 u32x2 __attribute__((ext_vector_type(2)));
typedef u32 u32x4 __attribute__((ext_vector_type(4)));
typedef int i32x4 __attribute__((ext_vector_type(4)));
#define LAS __attribute__((address_space(3)))
#define GAS __attribute__((address_space(1)))
#define RLX_AGENT __ATOMIC_RELAXED, __HIP_MEMORY_SCOPE_AGENT

constexpr int L_ = 2048, D_ = 512, N_ = 128, F_ = 1792;
#define CONV_SPLIT 0
#define S_PRIO 0
#define POLL_SLEEP 8
#ifndef M_PRIO_E1
#define M_PRIO_E1 2
#endif
#define PRETOUCH 0
#define ROT 0
#define CODE_PF_BYTES 24576

constexpr u32 WS_STAT = 4096;
constexpr u32 FSTR = 128;
constexpr u32 WS_FXN = 32768;
constexpr u32 WS_FACT = WS_FXN + 1024 * FSTR;
constexpr u32 WS_FY = WS_FACT + 2048 * FSTR;
constexpr u32 WS_XNT = 1048576;
constexpr u32 WS_ACT = WS_XNT + 2097152;
constexpr u32 WS_YT = WS_ACT + 7340032;
constexpr u32 WS_END = WS_YT + 2097152;
#define DBG_OFF (16u << 20)

constexpr u32 STAGE = 32768;
constexpr u32 KT = 131072;
constexpr u32 KTW = 4352;
constexpr u32 XT = 148480;
constexpr u32 KX = 148480;
constexpr u32 KRO = 148480;
constexpr u32 KRS = 4576;
constexpr u32 DUM = 157632;
constexpr u32 SYN = 158656;
constexpr u32 CSO = 158784;
constexpr u32 STO = 129024;
constexpr u32 B1O = 129536;
constexpr u32 KLIN = 161248;
constexpr u32 KLS = 1296;
constexpr u32 UC = 131072;
constexpr u32 UCS = 4160;
constexpr u32 CRED = 131584;
constexpr u32 YI = 131584;
constexpr u32 LDS_BYTES = 163840;
enum { C_LANDED = 0, E_XT = 16, E_XTR, E_XNST, E_KX, E_KLIN, E_UC, E_CONV, E_YST, E_CS, E_ACT, E_CRED, E_W2 };

constexpr int SPIN_CAP = 1 << 16;

__device__ __forceinline__ float bflo(u32 v) { return __uint_as_float(v << 16); }
__device__ __forceinline__ float bfhi(u32 v) { return __uint_as_float(v & 0xffff0000u); }
__device__ __forceinline__ u32 pkbf(float a, float b) {
  f32x2 v = {a, b};
  return __builtin_bit_cast(u32, __builtin_convertvector(v, b16x2));
}
__device__ __forceinline__ u32 pkh(float a, float b) {
  f32x2 v = {a, b};
  return __builtin_bit_cast(u32, __builtin_convertvector(v, h16x2));
}
__device__ __forceinline__ float rbf(float a) { return bflo(pkbf(a, 0.f)); }

__device__ __forceinline__ i32x4 mk_srd(const void* p, u32 bytes) {
  u64 a = (u64)p;
  i32x4 r;
  r[0] = (int)(u32)a;
  r[1] = (int)((u32)(a >> 32) & 0xffffu);
  r[2] = (int)bytes;
  r[3] = 0x00020000;
  return r;
}
__device__ __forceinline__ void dma16(i32x4 srd, u32 voff, u32 soff, u32 dst) {
  asm volatile("s_mov_b32 m0, %3\n\ts_nop 0\n\tbuffer_load_dwordx4 %0, %1, %2 offen lds"
               :: "v"(voff), "s"(srd), "s"(__builtin_amdgcn_readfirstlane(soff)), "s"(__builtin_amdgcn_readfirstlane(dst)));
}
__device__ __forceinline__ void dma4(i32x4 srd, u32 voff, u32 dst) {
  asm volatile("s_mov_b32 m0, %2\n\ts_nop 0\n\tbuffer_load_dword %0, %1, 0 offen lds"
               :: "v"(voff), "s"(srd), "s"(__builtin_amdgcn_readfirstlane(dst)));
}
__device__ __forceinline__ u32 ald4(i32x4 srd, u32 voff) {
  u32 r;
  asm volatile("buffer_load_dword %0, %1, %2, 0 offen" : "=v"(r) : "v"(voff), "s"(srd) : "memory");
  return r;
}
__device__ __forceinline__ u32x2 ald8(i32x4 srd, u32 voff) {
  u32x2 r;
  asm volatile("buffer_load_dwordx2 %0, %1, %2, 0 offen" : "=v"(r) : "v"(voff), "s"(srd) : "memory");
  return r;
}
__device__ __forceinline__ u32x4 ald16(i32x4 srd, u32 voff) {
  u32x4 r;
  asm volatile("buffer_load_dwordx4 %0, %1, %2, 0 offen" : "=v"(r) : "v"(voff), "s"(srd) : "memory");
  return r;
}
#define BAR() asm volatile("s_waitcnt lgkmcnt(0)\n\ts_barrier" ::: "memory")
#define WAITV(n) asm volatile("s_waitcnt vmcnt(" #n ")" ::: "memory")
#define SB() __builtin_amdgcn_sched_barrier(0)

__device__ __forceinline__ f32x4 mfma_bf(u32x4 a, u32x4 b, f32x4 c) {
  return __builtin_amdgcn_mfma_f32_16x16x32_bf16(__builtin_bit_cast(b16x8, a), __builtin_bit_cast(b16x8, b), c, 0, 0, 0);
}
__device__ __forceinline__ f32x4 mfma_h(u32x4 a, u32x4 b, f32x4 c) {
  return __builtin_amdgcn_mfma_f32_16x16x32_f16(__builtin_bit_cast(h16x8, a), __builtin_bit_cast(h16x8, b), c, 0, 0, 0);
}
__device__ __forceinline__ u32x2 tr_rd(LAS char* p) {
  return __builtin_bit_cast(u32x2, __builtin_amdgcn_ds_read_tr16_b64_v4i16((LAS s16x4*)p));
}
__device__ __forceinline__ u32x4 cat(u32x2 a, u32x2 b) { return u32x4{a[0], a[1], b[0], b[1]}; }
template <int CTRL> __device__ __forceinline__ float dpp_mov(float v) {
  return __builtin_bit_cast(float, __builtin_amdgcn_update_dpp(0, __builtin_bit_cast(int, v), CTRL, 0xf, 0xf, true));
}
__device__ __forceinline__ float rdlane(float v, int i) {
  return __builtin_bit_cast(float, __builtin_amdgcn_readlane(__builtin_bit_cast(int, v), i));
}
__device__ __forceinline__ float wave_max(float v) {
  v = fmaxf(v, dpp_mov<0xB1>(v));
  v = fmaxf(v, dpp_mov<0x4E>(v));
  v = fmaxf(v, dpp_mov<0x141>(v));
  v = fmaxf(v, dpp_mov<0x140>(v));
  return fmaxf(fmaxf(rdlane(v, 0), rdlane(v, 16)), fmaxf(rdlane(v, 32), rdlane(v, 48)));
}
__device__ __forceinline__ float wave_sum(float v) {
  v += dpp_mov<0xB1>(v);
  v += dpp_mov<0x4E>(v);
  v += dpp_mov<0x141>(v);
  v += dpp_mov<0x140>(v);
  return (rdlane(v, 0) + rdlane(v, 16)) + (rdlane(v, 32) + rdlane(v, 48));
}
__device__ __forceinline__ u64 probe(GAS u64* p) { return __hip_atomic_load(p, RLX_AGENT); }
__device__ __forceinline__ void wait_flags(GAS u64* p, u64 v, u64 tag) {
  if (!__all(v == tag)) {
#pragma unroll 1
    for (int it = 0; it < SPIN_CAP; ++it) {
      __builtin_amdgcn_s_sleep(POLL_SLEEP);
      v = __hip_atomic_load(p, RLX_AGENT);
      if (__all(v == tag)) break;
    }
  }
  __builtin_amdgcn_fence(__ATOMIC_ACQUIRE, "wavefront");
}
template <int I, int N, class F> __device__ __forceinline__ void static_for(F&& f) {
  if constexpr (I < N) { f(std::integral_constant<int, I>{}); static_for<I + 1, N>(f); }
}
#define CV(x) decltype(x)::value
__device__ __forceinline__ void lds_arrive(u32 addr) {
  u32 one = 1;
  asm volatile("s_mov_b64 exec, 1\n\tds_add_u32 %0, %1\n\ts_mov_b64 exec, -1" :: "v"(addr), "v"(one) : "memory");
}
__device__ __forceinline__ void lds_wait(LAS char* p, u32 target) {
#pragma unroll 1
  for (int it = 0; it < (1 << 22); ++it) {
    u32 v = *(volatile LAS u32*)p;
    if ((u32)__builtin_amdgcn_readfirstlane(v) >= target) break;
    __builtin_amdgcn_s_sleep(1);
  }
  asm volatile("" ::: "memory");
}
__device__ __forceinline__ void lds_spin(LAS char* p, u32 target) {
#pragma unroll 1
  for (int it = 0; it < (1 << 24); ++it) {
    u32 v = *(volatile LAS u32*)p;
    if ((u32)__builtin_amdgcn_readfirstlane(v) >= target) break;
  }
  asm volatile("" ::: "memory");
}
__device__ __forceinline__ LAS char* opq(LAS char* p) { asm volatile("" : "+v"(p)); return p; }
__device__ __forceinline__ float half_sum(float v) {
  v += dpp_mov<0xB1>(v);
  v += dpp_mov<0x4E>(v);
  v += dpp_mov<0x141>(v);
  v += dpp_mov<0x140>(v);
  auto r = __builtin_amdgcn_permlane16_swap(__builtin_bit_cast(u32, v), __builtin_bit_cast(u32, v), false, false);
  return __builtin_bit_cast(float, (u32)r[0]) + __builtin_bit_cast(float, (u32)r[1]);
}
struct cpx { float re, im; };
__device__ __forceinline__ cpx cmul(cpx a, cpx b) {
  cpx r;
  r.re = fmaf(a.re, b.re, -(a.im * b.im));
  r.im = fmaf(a.re, b.im, a.im * b.re);
  return r;
}

#define STAMP(k)

struct Params {
  const u16 *x, *Are, *Aim, *Bre, *Bim, *Cre, *Cim, *logdt, *dskip, *w1, *b1, *w2;
  u16* out;
  char* ws;
};

__global__ void __launch_bounds__(512, 2) fused_kernel(Params P) {
  extern __shared__ __attribute__((aligned(16))) char smem[];
  LAS char* S = (LAS char*)smem;
  const u32 sbase = (u32)(size_t)S;
  char* ws = P.ws;
  const int t = __builtin_amdgcn_workitem_id_x(), l = t & 63, w = __builtin_amdgcn_readfirstlane(t >> 6);
  const int h = l >> 4, l15 = l & 15;
  const int b = __builtin_amdgcn_workgroup_id_x(), xcd = b & 7, rk = b >> 3;
  const int bi = 4 * (xcd >> 1) + (rk >> 3), bj = 8 * (xcd & 1) + (rk & 7);
  const int bp = 4 * xcd + (rk >> 3), bc = rk & 7;
  const int q4 = w & 3;
  STAMP(0);
  if (w == 7) dma16(mk_srd((const void*)__builtin_amdgcn_s_getpc(), CODE_PF_BYTES), rk * 1024 + l * 16, 0, sbase + DUM);

  const __amdgpu_buffer_rsrc_t rs_ws = __builtin_amdgcn_make_buffer_rsrc((void*)ws, 0, WS_END, 0x00020000);
  const __amdgpu_buffer_rsrc_t rs_x = __builtin_amdgcn_make_buffer_rsrc((void*)P.x, 0, L_ * D_ * 2, 0x00020000);
  const __amdgpu_buffer_rsrc_t rs_out = __builtin_amdgcn_make_buffer_rsrc((void*)P.out, 0, L_ * D_ * 2, 0x00020000);
  const i32x4 sx = mk_srd(P.x, L_ * D_ * 2), sw1 = mk_srd(P.w1, D_ * F_ * 2), sw2 = mk_srd(P.w2, D_ * F_ * 2);
  u64 tag;
  u32 ldt2, dsk2;
  asm volatile("s_load_dwordx2 %0, %3, 0x0\n\ts_load_dword %1, %4, 0x0\n\ts_load_dword %2, %5, 0x0"
               : "=&s"(tag), "=&s"(ldt2), "=&s"(dsk2) : "s"(ws), "s"(P.logdt + 2 * b), "s"(P.dskip + 2 * b));

  u32 p_ar, p_ai, p_br, p_bi, p_cr, p_ci;
  u32x4 sr0, sr1;
  const int cc = q4 >> 1, hm = q4 & 1, ch = 2 * b + cc;
  if (w < 4) {
    if (t < 32) *(LAS u32*)(S + SYN + t * 4) = 0;
  } else {
    const u32 o = (128 * bi + 8 * bj + 2 * q4) * 1024 + l * 32;
    sr0 = ald16(sx, o);
    sr1 = ald16(sx, o + 16);
    const u32 po = ch * 256 + 4 * l;
    p_ar = ald4(mk_srd(P.Are, D_ * N_ * 2), po);
    p_ai = ald4(mk_srd(P.Aim, D_ * N_ * 2), po);
    p_br = ald4(mk_srd(P.Bre, D_ * N_ * 2), po);
    p_bi = ald4(mk_srd(P.Bim, D_ * N_ * 2), po);
    p_cr = ald4(mk_srd(P.Cre, D_ * N_ * 2), po);
    p_ci = ald4(mk_srd(P.Cim, D_ * N_ * 2), po);
  }
  const u32 dq = q4 < 3 ? 4096u : 12288u, dq2 = q4 < 3 ? 11264u : 12288u;
  const u32 vx = (128 * bi + 8 * q4 + (l >> 3)) * 1024 + (((l & 7) ^ (l >> 3)) * 16);
  u32 vwa, vwb;
  {
    const u32 g0 = q4 * 64 + l, pos0 = (g0 * 4682u) >> 16, c0 = g0 - pos0 * 14;
    const u32 g1 = g0 + 256, pos1 = (g1 * 4682u) >> 16, c1 = g1 - pos1 * 14;
    const u32 k0 = (pos0 & ~12u) | ((pos0 & 4) << 1) | ((pos0 & 8) >> 1);
    const u32 k1 = (pos1 & ~12u) | ((pos1 & 4) << 1) | ((pos1 & 8) >> 1);
    vwa = k0 * (F_ * 2) + bj * 224 + c0 * 16;
    vwb = q4 < 3 ? k1 * (F_ * 2) + bj * 224 + c1 * 16 : 0x7ff00000u;
  }
  auto issue_stage = [&](int c) {
    const u32 sb_ = sbase + c * STAGE + q4 * 1024;
#pragma unroll
    for (int i = 0; i < 4; ++i) dma16(sx, vx, c * 128 + i * 32768, sb_ + i * 4096);
    const u32 so = c * (64 * F_ * 2);
    dma16(sw1, vwa, so, sb_ + 16384);
    dma16(sw1, vwa, so + 32 * F_ * 2, sb_ + 16384 + 7168);
    dma16(sw1, vwb, so, sb_ + 16384 + dq);
    dma16(sw1, vwb, so + 32 * F_ * 2, sb_ + 16384 + dq2);
  };
  if (w < 4) issue_stage(0);
  BAR();
  asm volatile("" : "+s"(tag), "+s"(ldt2), "+s"(dsk2));
  tag += 1ull;
  if (w < 4) { issue_stage(1); issue_stage(2); }
  u32 w2o;
  LAS char* w2a[4];
  {
    const int rr = l >> 3, slot = l & 7;
    const int dt = (slot >> 1) ^ ((rr >> 1) & 3);
    w2o = (224 * w + rr) * 1024 + bc * 128 + (2 * dt + (slot & 1)) * 16;
    const int q = l15 >> 2, p = l & 3, x = (2 * h + (q >> 1)) & 3;
#pragma unroll
    for (int d = 0; d < 4; ++d) w2a[d] = S + 4096 * w + (4 * h + q) * 128 + ((d ^ x) * 32) + (p >> 1) * 16 + (p & 1) * 8;
  }
#define W2TILE(ks, hf) ((ks) < 3 ? 2 * (ks) + (hf) : ((ks) == 3 ? ((hf) ? 13 : 6) : 7 + 2 * ((ks) - 4) + (hf)))
#define ISSUE_W2(p, n, slot) dma16(sw2, w2o, (W2TILE(p, (n) >> 1) * 16 + ((n) & 1) * 8) * 1024, sbase + (slot) * STAGE + 4096 * w + (n) * 1024)
  auto w2_frag = [&](int d, int slot) {
    LAS char* p = slot >= 2 ? opq(w2a[d] + 2 * STAGE) + (slot - 2) * STAGE : w2a[d] + slot * STAGE;
    return cat(tr_rd(p), tr_rd(p + 2048));
  };
  u32x4 wr1[4][4];
  auto w2_regs = [&]() {
#pragma unroll
    for (int k = 0; k < 4; ++k)
#pragma unroll
      for (int d = 0; d < 4; ++d) wr1[k][d] = w2_frag(d, k);
    asm volatile("s_waitcnt lgkmcnt(0)" ::: "memory");
  };
  auto w2_req = [&]() {
#pragma unroll
    for (int n = 0; n < 4; ++n) ISSUE_W2(2, n, 0);
#pragma unroll
    for (int n = 0; n < 4; ++n) ISSUE_W2(3, n, 1);
#pragma unroll
    for (int n = 0; n < 4; ++n) ISSUE_W2(6, n, 2);
  };
  f32x4 ca[4];
  const u32 syn = sbase + SYN;
  LAS char* synp = opq(S + SYN);
#define ARRIVE(e) lds_arrive(syn + (e) * 4)
#define AWAIT(e) lds_wait(synp + (e) * 4, 4)

  if (w < 4) {
    f32x4 acc[7][2], accs[7];
#pragma unroll
    for (int f = 0; f < 7; ++f) { acc[f][0] = f32x4{0, 0, 0, 0}; acc[f][1] = f32x4{0, 0, 0, 0}; accs[f] = f32x4{0, 0, 0, 0}; }
    u32 xa = (32 * q4 + l15) * 128 + ((h ^ (l & 7)) * 16);
    u32 wo = 16384 + (16 * (h >> 1) + 4 * (h & 1) + (l15 >> 2)) * 224 + (l & 3) * 8;
    u32x4 ones = {0x3f803f80u, 0x3f803f80u, 0x3f803f80u, 0x3f803f80u};
    asm volatile("" : "+v"(ones));
    u32x4 xf[2][2];
    u32x2 wl[2][7], wh[2][7];
    u32 lnd = 0;
    auto kstep = [&](auto kc, auto mc, u32 so, u32 dst, u32 cnt_addr, LAS char* cnt_p, u32 tgt, int nwait) {
      constexpr int k = CV(kc), n = k ^ 1, MODE = CV(mc);
      constexpr int r0 = k ? 4 : 0;
      LAS char* xp = S + (k ? xa : (xa ^ 64));
      LAS char* wp = S + wo + (k ? 0 : 32 * 224);
      static_for<0, 14>([&](auto jc) {
        constexpr int j = CV(jc), f = j >> 1, mt = j & 1;
        SB();
        acc[f][mt] = mfma_bf(cat(wl[k][f], wh[k][f]), xf[k][mt], acc[f][mt]);
        SB();
        if constexpr (k == 1 && j == 1) lnd = *(volatile LAS u32*)cnt_p;
        if constexpr (k == 1 && j == 3) { if ((u32)__builtin_amdgcn_readfirstlane(lnd) < tgt) lds_spin(cnt_p, tgt); }
        if constexpr (j == r0) { xf[n][0] = *(LAS u32x4*)(xp); xf[n][1] = *(LAS u32x4*)(xp + 2048); }
        if constexpr (j >= r0 + 1 && j <= r0 + 7) {
          wl[n][j - r0 - 1] = tr_rd(wp + (j - r0 - 1) * 32);
          wh[n][j - r0 - 1] = tr_rd(wp + 8 * 224 + (j - r0 - 1) * 32);
        }
        constexpr int pc = j == (k ? 5 : 1) ? 0 : j == (k ? 8 : 5) ? 1 : j == (k ? 11 : 10) ? 2 : j == (k ? 13 : 12) ? 3 : -1;
        if constexpr (pc >= 0 && MODE == 1 && k == 0)
          dma16(sw1, pc < 2 ? vwa : vwb, so + (pc & 1) * (32 * F_ * 2), dst + (pc == 0 ? 0u : pc == 1 ? 7168u : pc == 2 ? dq : dq2));
        if constexpr (pc >= 0 && MODE == 1 && k == 1) dma16(sx, vx, so + pc * 32768, dst + pc * 4096);
        if constexpr (pc >= 0 && MODE == 2) dma16(sw2, w2o, so + pc * 8192, dst + pc * 1024);
        if constexpr (k == 0 && j == 8) {
          if constexpr (MODE == 1) WAITV(10); else { if (nwait) WAITV(10); }
          lds_arrive(cnt_addr);
        }
      });
      SB();
    };
    auto colsum = [&](auto kc) {
#pragma unroll
      for (int f = 0; f < 7; ++f) accs[f] = mfma_bf(cat(wl[CV(kc)][f], wh[CV(kc)][f]), ones, accs[f]);
    };
    using I0 = std::integral_constant<int, 0>; using I1 = std::integral_constant<int, 1>; using I2 = std::integral_constant<int, 2>;
    WAITV(16);
    ARRIVE(C_LANDED);
    lds_spin(synp + C_LANDED * 4, 4);
    xf[0][0] = *(LAS u32x4*)(S + xa); xf[0][1] = *(LAS u32x4*)(S + xa + 2048);
#pragma unroll
    for (int f = 0; f < 7; ++f) { wl[0][f] = tr_rd(S + wo + f * 32); wh[0][f] = tr_rd(S + wo + 8 * 224 + f * 32); }
    auto adv = [&]() { xa = (xa + STAGE) & 131071u; wo = (wo + STAGE) & 131071u; };
#pragma unroll 1
    for (int c = 0; c < 5; ++c) {
      const u32 sl = sbase + ((c + 3) & 3) * STAGE + q4 * 1024;
      kstep(I0{}, I1{}, (c + 3) * (64 * F_ * 2), sl + 16384, syn + (c + 1) * 4, synp, 0, 10);
      if (((2 * c) & 3) == q4) colsum(I0{});
      adv();
      kstep(I1{}, I1{}, (c + 3) * 128, sl, 0, synp + (c + 1) * 4, 4, 0);
      if (((2 * c + 1) & 3) == q4) colsum(I1{});
    }
#pragma unroll 1
    for (int c = 5; c < 8; ++c) {
      const u32 sl = sbase + (c - 5) * STAGE + 4096 * w, wso = c < 7 ? (c - 5) * 32768 : 7 * 16384;
      kstep(I0{}, I2{}, wso, sl, syn + (c + 1) * 4, synp, 0, c < 7);
      if (((2 * c) & 3) == q4) colsum(I0{});
      adv();
      kstep(I1{}, I2{}, wso + 896 * 1024, sl + 16384, 0, synp + (c + 1) * 4, c < 7 ? 4u : 0u, 0);
      if (((2 * c + 1) & 3) == q4) colsum(I1{});
    }
    if (l15 == 0) {
      LAS char* cp = opq(S + CSO + q4 * 448 + h * 16);
#pragma unroll
      for (int f = 0; f < 7; ++f) *(LAS f32x4*)(cp + 64 * f) = accs[f];
    }
    AWAIT(E_XT);
    u32 pay2[2];
    u32x2 b1v[7];
    {
      LAS char* sp = opq(S + STO + (32 * q4 + l15) * 4);
      LAS char* bp_ = opq(S + B1O + h * 8);
      pay2[0] = *(LAS u32*)sp; pay2[1] = *(LAS u32*)(sp + 64);
#pragma unroll
      for (int f = 0; f < 7; ++f) b1v[f] = *(LAS u32x2*)(bp_ + 32 * f);
    }
    ARRIVE(E_CS);
    const float LOG2E = 1.4426950408889634f, LN2 = 0.6931471805599453f;
    float rs[2], tm[2];
#pragma unroll
    for (int mt = 0; mt < 2; ++mt) {
      rs[mt] = __builtin_amdgcn_rcpf(bfhi(pay2[mt])) * LOG2E;
      tm[mt] = -bflo(pay2[mt]) * rs[mt];
    }
    lds_spin(synp + E_CS * 4, 4);
    __builtin_amdgcn_s_setprio(M_PRIO_E1);
    const int jp = bj & 1;
    const u32 abase = WS_ACT + ((8 * bi + 2 * q4) * 56 + 7 * (bj >> 1) + (jp ? 4 : 0)) * 1024 + l * 16;
    LAS char* csp = opq(S + CSO + h * 16);
    u32x2 pk[2][2];
    static_for<0, 7>([&](auto fc) {
      constexpr int f = CV(fc);
      const f32x4 cs = (*(LAS f32x4*)(csp + 64 * f) + *(LAS f32x4*)(csp + 64 * f + 448)) + (*(LAS f32x4*)(csp + 64 * f + 896) + *(LAS f32x4*)(csp + 64 * f + 1344));
      const float bia[4] = {bflo(b1v[f][0]) * LOG2E, bfhi(b1v[f][0]) * LOG2E, bflo(b1v[f][1]) * LOG2E, bfhi(b1v[f][1]) * LOG2E};
#pragma unroll
      for (int mt = 0; mt < 2; ++mt) {
        float a[4];
#pragma unroll
        for (int r = 0; r < 4; ++r) {
          float g = fmaf(acc[f][mt][r], rs[mt], fmaf(tm[mt], cs[r], bia[r]));
          float s1 = __builtin_amdgcn_exp2f(g) + 1.f;
          float rr = __builtin_amdgcn_rcpf(fmaf(s1, s1, 1.f));
          a[r] = g * fmaf(rr, -2.f * LN2, LN2);
        }
        pk[f & 1][mt] = u32x2{pkbf(a[0], a[1]), pkbf(a[2], a[3])};
        if constexpr (f < 2) {
          SB();
          const int n = 2 * f + mt;
          const u32 so = 9 * 16384 + n * 8192, dd = sbase + 3 * STAGE + 4096 * w + n * 1024;
          dma16(sw2, w2o, so, dd); dma16(sw2, w2o, so + 896 * 1024, dd + 16384);
          SB();
        }
      }
      if constexpr (f == 5) {
        WAITV(0);
        if (l == 0) __hip_atomic_store((GAS u64*)(ws + WS_FACT + ((bi * 16 + bj) * 8 + q4) * FSTR), tag, RLX_AGENT);
        ARRIVE(E_W2);
      }
      if constexpr (f & 1) {
#pragma unroll
        for (int mt = 0; mt < 2; ++mt)
          __builtin_amdgcn_raw_buffer_store_b128(cat(pk[0][mt], pk[1][mt]), rs_ws, abase + (f >> 1) * 1024, mt * 56 * 1024, 16);
      }
      if constexpr (f == 6) {
#pragma unroll
        for (int mt = 0; mt < 2; ++mt)
          __builtin_amdgcn_raw_buffer_store_b64(pk[0][mt], rs_ws, abase + (jp ? -1024 + 8 : 3072), mt * 56 * 1024, 16);
      }
    });
    w2_regs();
    WAITV(0);
    if (l == 0) __hip_atomic_store((GAS u64*)(ws + WS_FACT + ((bi * 16 + bj) * 8 + 4 + q4) * FSTR), tag, RLX_AGENT);
    __builtin_amdgcn_s_setprio(0);
    w2_req();
  } else {
    const int ts = t - 256;
    __builtin_amdgcn_s_setprio(S_PRIO);
    WAITV(0);
    asm volatile("" : "+v"(p_ar), "+v"(p_ai), "+v"(p_br), "+v"(p_bi), "+v"(p_cr), "+v"(p_ci));
    asm volatile("" : "+v"(sr0), "+v"(sr1));
    {
      float e[16];
#pragma unroll
      for (int q = 0; q < 4; ++q) { e[2 * q] = bflo(sr0[q]); e[2 * q + 1] = bfhi(sr0[q]); e[8 + 2 * q] = bflo(sr1[q]); e[9 + 2 * q] = bfhi(sr1[q]); }
      float s0 = (e[0] + e[1]) + (e[2] + e[3]), s1 = (e[4] + e[5]) + (e[6] + e[7]), s2 = (e[8] + e[9]) + (e[10] + e[11]), s3 = (e[12] + e[13]) + (e[14] + e[15]);
      const float mean = half_sum((s0 + s1) + (s2 + s3)) * (1.0f / D_);
      float v0 = 0, v1 = 0;
#pragma unroll
      for (int q = 0; q < 8; ++q) { float d0 = e[q] - mean, d1 = e[8 + q] - mean; v0 = fmaf(d0, d0, v0); v1 = fmaf(d1, d1, v1); }
      const float var = half_sum(v0 + v1) * (1.0f / D_);
      const float sd = __builtin_amdgcn_sqrtf(rbf(rbf(var) + 1e-5f));
      const u32 pay = pkbf(mean, sd);
      if ((l & 31) == 0) __hip_atomic_store((GAS u64*)(ws + WS_STAT + (128 * bi + 8 * bj + 2 * q4 + (l >> 5)) * 8), ((u64)(u32)tag << 32) | pay, RLX_AGENT);
    }
    u32x4 xs0 = ald16(sx, (128 * bi + (ts >> 1)) * 1024 + bj * 64 + (ts & 1) * 32);
    u32x4 xs1 = ald16(sx, (128 * bi + (ts >> 1)) * 1024 + bj * 64 + (ts & 1) * 32 + 16);
    u32 b1w = ald4(mk_srd(P.b1, F_ * 2), bj * 224 + 4 * l);
    float yscale, gmax, zr, zi;
    cpx cbn;
    {
      const float LOG2E = 1.4426950408889634f, INV2PI = 0.15915494309189535f;
      const float lx = cc ? bfhi(ldt2) : bflo(ldt2);
      const float th = lx * LOG2E, tl = fmaf(lx, LOG2E, -th) + lx * 1.925963033500011e-8f;
      const float dt = rbf(__builtin_amdgcn_exp2f(th) * fmaf(tl, 0.6931471805599453f, 1.0f));
      cpx cb0 = cmul(cpx{bflo(p_cr), bflo(p_ci)}, cpx{dt * bflo(p_br), dt * bflo(p_bi)});
      cpx cb1 = cmul(cpx{bfhi(p_cr), bfhi(p_ci)}, cpx{dt * bfhi(p_br), dt * bfhi(p_bi)});
      const float z0 = dt * bflo(p_ar) * LOG2E, z1 = dt * bfhi(p_ar) * LOG2E;
      gmax = fmaxf(wave_max(fmaxf(z0, z1)), 0.f);
      const float cbmax = wave_max(fmaxf(fmaxf(fabsf(cb0.re), fabsf(cb0.im)), fmaxf(fabsf(cb1.re), fabsf(cb1.im))));
      const float cbs = __uint_as_float((__float_as_uint(cbmax) + 0x007fffffu) & 0x7f800000u);
      const float icb = cbs > 0.f ? __builtin_amdgcn_rcpf(cbs) : 0.f;
      yscale = cbs * __builtin_amdgcn_exp2f(gmax * 2048.f) * (1.0f / 64.f);
      zr = fmaxf((hm ? z1 : z0) - gmax, -1e30f);
      zi = dt * (hm ? bfhi(p_ai) : bflo(p_ai)) * INV2PI;
      cbn = hm ? cb1 : cb0;
      cbn.re *= 64.f * icb; cbn.im *= -64.f * icb;
    }
    auto cexp = [&](float a) {
      float mg = __builtin_amdgcn_exp2f(zr * a);
      float ph = __builtin_amdgcn_fractf(zi * a);
      return cpx{mg * __builtin_amdgcn_cosf(ph), mg * __builtin_amdgcn_sinf(ph)};
    };
    const cpx e1 = cexp(1.f), e8 = cexp(8.f), e128 = cexp(128.f);
    GAS u64* stat_p = (GAS u64*)(ws + WS_STAT + (128 * bi + (ts >> 1)) * 8);
    LAS char* tb = opq(S + KT + q4 * KTW + l * 4);
    LAS char* tf = opq(S + KT + q4 * KTW + l15 * 272 + h * 16);
    auto put = [&](int row, cpx v) { *(volatile LAS u32*)(tb + row * 272) = pkh(v.re, v.im); };
    u32x4 fa[4];
    f32x4 kacc[3];
    auto tile_mma = [&]() {
      f32x4 a = {0, 0, 0, 0};
#pragma unroll
      for (int k = 0; k < 4; ++k) a = mfma_h(fa[k], *(volatile LAS u32x4*)(tf + k * 64), a);
      return a;
    };
    {
      cpx wv = cbn;
      const cpx e8c = cpx{e8.re, -e8.im};
#pragma unroll
      for (int q = 0; q < 16; ++q) { put(q, wv); if (q < 15) wv = cmul(wv, e8c); }
#pragma unroll
      for (int k = 0; k < 4; ++k) fa[k] = *(volatile LAS u32x4*)(tf + k * 64);
    }
    u64 stat_v = probe(stat_p);
    cpx e2, e4, e6;
    {
      cpx lo = cpx{1.f, 0.f}, g = e128;
#pragma unroll
      for (int q = 0; q < 8; ++q) {
        put(q, lo); put(8 + q, g);
        if (q == 2) e2 = lo;
        if (q == 4) e4 = lo;
        if (q == 6) e6 = lo;
        if (q < 7) { lo = (q == 0) ? e1 : cmul(lo, e1); g = cmul(g, e1); }
      }
      kacc[0] = tile_mma();
    }
    if (!__all((u32)(stat_v >> 32) == (u32)tag)) {
#pragma unroll 1
      for (int it = 0; it < SPIN_CAP; ++it) {
        __builtin_amdgcn_s_sleep(16);
        stat_v = probe(stat_p);
        if (__all((u32)(stat_v >> 32) == (u32)tag)) break;
      }
    }
    WAITV(0);
    asm volatile("" : "+v"(xs0), "+v"(xs1), "+v"(b1w));
    {
      const u32 pay = (u32)stat_v;
      const float mb = bflo(pay), rsd = __builtin_amdgcn_rcpf(bfhi(pay));
      *(LAS u32*)(S + STO + (ts >> 1) * 4) = pay;
      *(LAS u32*)(S + B1O + l * 4) = b1w;
      LAS char* xt = opq(S + XT + (ts & 1) * 16 * 272 + (ts >> 1) * 2);
#pragma unroll
      for (int q = 0; q < 8; ++q) {
        const u32 xv = q < 4 ? xs0[q] : xs1[q - 4];
        u32 dd = pkbf(bflo(xv) - mb, bfhi(xv) - mb);
        u32 xn = pkbf(bflo(dd) * rsd, bfhi(dd) * rsd);
        *(LAS u16*)(xt + (2 * q) * 272) = (u16)xn;
        *(LAS u16*)(xt + (2 * q + 1) * 272) = (u16)(xn >> 16);
      }
      ARRIVE(E_XT);
    }
    cpx hv = cmul(e128, e128);
#pragma unroll
    for (int a = 2; a < 8; ++a) {
      if (a < 4) {
        put((a - 2) * 4 + 0, hv); put((a - 2) * 4 + 1, cmul(hv, e2));
        put((a - 2) * 4 + 2, cmul(hv, e4)); put((a - 2) * 4 + 3, cmul(hv, e6));
      } else {
        put(8 + (a - 4) * 2 + 0, hv); put(8 + (a - 4) * 2 + 1, cmul(hv, e4));
      }
      hv = cmul(hv, e128);
      if (a == 2) {
          AWAIT(E_XT);
          LAS char* xr = opq(S + XT + (ts >> 4) * 272 + (ts & 15) * 16);
          u32x4 v0 = *(LAS u32x4*)(xr), v1 = *(LAS u32x4*)(xr + 16 * 272);
          const u32 xo = WS_XNT + ((32 * bj + (ts >> 4)) * 2048 + 128 * bi + (ts & 15) * 8) * 2;
          __builtin_amdgcn_raw_buffer_store_b128(v0, rs_ws, xo, 0, 16);
          __builtin_amdgcn_raw_buffer_store_b128(v1, rs_ws, xo, 16 * 4096, 16);
          ARRIVE(E_XTR);
      }
    }
    kacc[1] = tile_mma();
#pragma unroll
    for (int a = 8; a <= 16; ++a) { put(a - 8, hv); if (a < 16) hv = cmul(hv, e128); }
    kacc[2] = tile_mma();
    WAITV(0);
    if (l == 0) __hip_atomic_store((GAS u64*)(ws + WS_FXN + ((bj * 16 + bi) * 4 + q4) * FSTR), tag, RLX_AGENT);
    AWAIT(E_XTR);
    {
      LAS char* kx = opq(S + KX + q4 * 2048 + l * 16);
      *(LAS f32x4*)(kx) = hm ? kacc[0] : kacc[1];
      *(LAS f32x4*)(kx + 1024) = kacc[2];
      ARRIVE(E_KX);
      const int e = hm ? 1 + (l15 >> 3) : 0, lg = 3 - e, r8 = hm ? (l15 & 7) : l15;
      const int idx = (e ? 128 + 128 * e : 0) + ((r8 >> lg) << (7 - e)) + (r8 & ((1 << lg) - 1)) + ((4 * h) << lg);
      const int id2 = min(512 + 16 * l15 + 4 * h + 2 * hm, 642);
      LAS char* kl = S + KLIN + cc * KLS;
      AWAIT(E_KX);
      LAS char* ko_ = opq(S + KX + (q4 ^ 1) * 2048 + l * 16);
      f32x4 ka = (hm ? kacc[1] : kacc[0]) + *(LAS f32x4*)(ko_);
      f32x4 kb = kacc[2] + *(LAS f32x4*)(ko_ + 1024);
      float k2a = hm ? kb[2] : kb[0], k2b = hm ? kb[3] : kb[1];
      if (gmax > 0.f) {
#pragma unroll
        for (int r = 0; r < 4; ++r) ka[r] *= __builtin_amdgcn_exp2f(gmax * (float)(((idx + (r << lg) - 128 * e) << e) - 2048));
        k2a *= __builtin_amdgcn_exp2f(gmax * (float)(((id2 - 384) << 3) - 2048));
        k2b *= __builtin_amdgcn_exp2f(gmax * (float)(((id2 - 383) << 3) - 2048));
      }
#pragma unroll
      for (int r = 0; r < 4; ++r) *(LAS _Float16*)(kl + (idx + (r << lg)) * 2) = (_Float16)ka[r];
      *(LAS u32*)(kl + id2 * 2) = pkh(k2a, k2b);
    }
    ARRIVE(E_KLIN);
    GAS u64* fxn_p = (GAS u64*)(ws + WS_FXN + (((ch >> 5) * 16 + l15) * 4 + ((ch & 15) >> 2)) * FSTR);
    u64 fxn_v = probe(fxn_p);
    const int tc = hm * 64 + l;
    AWAIT(E_KLIN);
    {
      LAS char* kl = S + KLIN + cc * KLS;
      const float jf = (float)(2047 - 16 * tc);
      const bool low = tc >= 120;
      u32 ko[8];
#pragma unroll
      for (int q = 0; q < 8; ++q) {
        const u32 bits = pkbf(jf - (float)(2 * q), jf - (float)(2 * q + 1));
        const u32 j0 = 2047 - 16 * tc - 2 * q;
        const u32 pr = low ? (j0 | ((j0 - 1) << 16)) : bits - 0x42804280u;
        ko[q] = (u32)*(LAS u16*)(kl + (pr & 0xffff) * 2) | ((u32)*(LAS u16*)(kl + (pr >> 16) * 2) << 16);
      }
      LAS char* kr = opq(S + KRO + cc * KRS + tc * 32);
      *(LAS u32x4*)(kr) = u32x4{ko[0], ko[1], ko[2], ko[3]};
      *(LAS u32x4*)(kr + 16) = u32x4{ko[4], ko[5], ko[6], ko[7]};
      *(LAS u32x4*)(S + KRO + cc * KRS + 4096 + min(tc, 29) * 16) = u32x4{0, 0, 0, 0};
    }
    wait_flags(fxn_p, fxn_v, tag);
    u32x2 us[4];
    const u32 yo = (ch * 2048 + 16 * l15 + 4 * h) * 2;
    const u32 t0 = hm ? 512u : 0u, t1 = hm ? 1024u : 1536u, t2 = hm ? 2560u : 2048u, t3 = hm ? 3072u : 3584u;
    {
      const u32 uo = WS_XNT + (ch * 2048 + tc * 16) * 2;
      u32x4 ua = __builtin_amdgcn_raw_buffer_load_b128(rs_ws, uo, 0, 16);
      u32x4 ub = __builtin_amdgcn_raw_buffer_load_b128(rs_ws, uo + 16, 0, 16);
      u32 uc = __builtin_amdgcn_raw_buffer_load_b32(rs_ws, uo + 32, 0, 16);
      us[0] = __builtin_amdgcn_raw_buffer_load_b64(rs_ws, yo, WS_XNT + t0, 16);
      us[1] = __builtin_amdgcn_raw_buffer_load_b64(rs_ws, yo, WS_XNT + t1, 16);
      us[2] = __builtin_amdgcn_raw_buffer_load_b64(rs_ws, yo, WS_XNT + t2, 16);
      us[3] = __builtin_amdgcn_raw_buffer_load_b64(rs_ws, yo, WS_XNT + t3, 16);
      u32 d[9], o[8];
#pragma unroll
      for (int q = 0; q < 4; ++q) { d[q] = pkh(bflo(ua[q]), bfhi(ua[q])); d[4 + q] = pkh(bflo(ub[q]), bfhi(ub[q])); }
      d[8] = pkh(bflo(uc), 0.f);
#pragma unroll
      for (int q = 0; q < 8; ++q) o[q] = __builtin_amdgcn_alignbit(d[q + 1], d[q], 16);
      LAS char* ucb = opq(S + UC + cc * 2 * UCS + (2 * tc + 2) * 16);
#pragma unroll
      for (int k = 0; k < 2; ++k) {
        *(LAS u32x4*)(ucb + k * 16) = u32x4{d[4 * k], d[4 * k + 1], d[4 * k + 2], d[4 * k + 3]};
        *(LAS u32x4*)(ucb + k * 16 + UCS) = u32x4{o[4 * k], o[4 * k + 1], o[4 * k + 2], o[4 * k + 3]};
      }
      if (tc == 0) {
        LAS char* z = ucb - 32;
        *(LAS u32x4*)(z) = u32x4{0, 0, 0, 0};
        *(LAS u32x4*)(z + UCS) = u32x4{0, 0, 0, 0};
        *(LAS u32x4*)(z + 16) = u32x4{0, 0, 0, 0};
        *(LAS u32x4*)(z + UCS + 16) = u32x4{0, 0, 0, d[0] << 16};
      }
    }
    ARRIVE(E_UC);
    AWAIT(E_UC);
    {
#pragma unroll
      for (int q = 0; q < 4; ++q) ca[q] = f32x4{0, 0, 0, 0};
      const int v = l15 + 1, sft = v & 1;
      LAS char* ub0 = S + UC + (cc * 2 + sft) * UCS + (8 * h + v - sft) * 2;
      LAS char* ub[4];
#pragma unroll
      for (int q = 0; q < 4; ++q) ub[q] = opq(ub0 + q * 1024 - 64);
      LAS char* kbase = opq(S + KRO + cc * KRS + (2032 - 1792 - 16 * l15 + 8 * h) * 2);
      auto cv = [&](auto Gc) {
        constexpr int G = CV(Gc);
        constexpr int TA = G ? 1 : 0, TB = G ? 2 : 3, TC = G ? 5 : 4, TD = G ? 6 : 7;
        u32x4 U[2][TD + 1], Kf[2][TD + 1];
        auto ldu = [&](int i, int r) {
          LAS u32* p = (LAS u32*)(ub[i >> 1] + (i & 1) * 512 + r * 64);
          return u32x4{p[0], p[1], p[2], p[3]};
        };
#pragma unroll
        for (int i = 0; i <= TD; ++i) { U[0][i] = ldu(i, 1); Kf[0][TD - i] = *(LAS u32x4*)(kbase + (7 - (TD - i)) * 512); }
#pragma unroll 1
        for (int rp = 0; rp < 4; ++rp) {
          static_for<0, 2>([&](auto pc) {
            constexpr int p = CV(pc);
            static_for<0, TD + 1>([&](auto ic) {
              constexpr int i = CV(ic);
              SB();
              ca[3] = mfma_h(U[p][i], Kf[p][TD - i], ca[3]);
              SB();
              U[p ^ 1][i] = ldu(i, p + 2);
              SB();
              if constexpr (i >= TD - TC) ca[2] = mfma_h(U[p][i - (TD - TC)], Kf[p][TD - i], ca[2]);
              SB();
              Kf[p ^ 1][TD - i] = *(LAS u32x4*)(kbase + (p + 1) * 64 + (7 - (TD - i)) * 512);
              SB();
              if constexpr (i >= TD - TB) ca[1] = mfma_h(U[p][i - (TD - TB)], Kf[p][TD - i], ca[1]);
              if constexpr (i >= TD - TA) ca[0] = mfma_h(U[p][i - (TD - TA)], Kf[p][TD - i], ca[0]);
            });
          });
          SB();
#pragma unroll
          for (int q = 0; q < 4; ++q) ub[q] = opq(ub[q] + 128);
          kbase = opq(kbase + 128);
        }
      };
      if (hm == 0) cv(std::integral_constant<int, 0>{}); else cv(std::integral_constant<int, 1>{});
    }
    const float dsk = cc ? bfhi(dsk2) : bflo(dsk2);
    u32 tsk[4][2];
#pragma unroll
    for (int q = 0; q < 4; ++q) {
      tsk[q][0] = pkbf(bflo(us[q][0]) * dsk, bfhi(us[q][0]) * dsk);
      tsk[q][1] = pkbf(bflo(us[q][1]) * dsk, bfhi(us[q][1]) * dsk);
    }
    ARRIVE(E_CONV);
#pragma unroll
    for (int q = 0; q < 4; ++q) {
      const f32x4 a = ca[q];
      u32 y01 = pkbf(a[0] * yscale, a[1] * yscale), y23 = pkbf(a[2] * yscale, a[3] * yscale);
      u32x2 o = {pkbf(bflo(y01) + bflo(tsk[q][0]), bfhi(y01) + bfhi(tsk[q][0])), pkbf(bflo(y23) + bflo(tsk[q][1]), bfhi(y23) + bfhi(tsk[q][1]))};
      __builtin_amdgcn_raw_buffer_store_b64(o, rs_ws, yo, WS_YT + (q == 0 ? t0 : q == 1 ? t1 : q == 2 ? t2 : t3), 16);
    }
    WAITV(0);
    if (l == 0) __hip_atomic_store((GAS u64*)(ws + WS_FY + (b * 4 + q4) * FSTR), tag, RLX_AGENT);
    if (b == 0 && w == 7) {
      GAS u64* p = (GAS u64*)(ws + WS_FXN + (size_t)l * 4 * FSTR);
      const u64 v0 = probe(p), v1 = probe(p + 64 * 4 * FSTR / 8), v2 = probe(p + 128 * 4 * FSTR / 8), v3 = probe(p + 192 * 4 * FSTR / 8);
      wait_flags(p, v0, tag); wait_flags(p + 64 * 4 * FSTR / 8, v1, tag);
      wait_flags(p + 128 * 4 * FSTR / 8, v2, tag); wait_flags(p + 192 * 4 * FSTR / 8, v3, tag);
      if (l == 0) __hip_atomic_store((GAS u64*)ws, tag, RLX_AGENT);
    }
    AWAIT(E_W2);
    w2_regs();
    w2_req();
    __builtin_amdgcn_s_setprio(0);
  }

  {
    GAS u64* fact_p = (GAS u64*)(ws + WS_FACT + (((bp >> 1) * 16 + 2 * w + (l & 1)) * 8 + 2 * (bp & 1) + ((l >> 1) & 1)) * FSTR);
    wait_flags(fact_p, probe(fact_p), tag);
    const u32 ao = WS_ACT + (4 * bp * 56 + 7 * w) * 1024 + l * 16;
    u32x4 af[7][4];
#define KORD(i) ((i) < 2 ? (i) : (i) < 4 ? (i) + 2 : (i) < 6 ? (i) - 2 : 6)
    auto lda = [&](auto ic, auto lc) {
      af[CV(ic)][CV(lc)] = __builtin_amdgcn_raw_buffer_load_b128(rs_ws, ao, (CV(lc) * 56 + KORD(CV(ic))) * 1024, 16);
    };
    static_for<0, 3>([&](auto ic) { static_for<0, 4>([&](auto lc) { lda(ic, lc); }); });
    u64 fact_v = probe(fact_p + 4 * FSTR / 8);
    f32x4 ac[4][4];
#pragma unroll
    for (int dt = 0; dt < 4; ++dt)
#pragma unroll
      for (int lt = 0; lt < 4; ++lt) ac[dt][lt] = f32x4{0, 0, 0, 0};
    const int yT = bp >> 2;
    GAS u64* fy_p = (GAS u64*)(ws + WS_FY + ((32 * bc + 8 * q4 + ((l >> 1) & 7)) * 4 + (l & 1) * 2 + ((yT ^ (yT >> 1)) & 1)) * FSTR);
    u64 fy_v = 0;
    u32x4 xres;
    static_for<0, 7>([&](auto ic) {
      constexpr int i = CV(ic);
      u32x4 wf[4];
#pragma unroll
      for (int dt = 0; dt < 4; ++dt) {
        if constexpr (i < 4) wf[dt] = wr1[i][dt];
        else wf[dt] = w2_frag(dt, i - 4);
      }
      if constexpr (i == 1) wait_flags(fact_p + 4 * FSTR / 8, fact_v, tag);
      static_for<0, 4>([&](auto lc) {
        constexpr int lt = CV(lc);
        SB();
#pragma unroll
        for (int dt = 0; dt < 4; ++dt) ac[dt][lt] = mfma_bf(wf[dt], af[i][lt], ac[dt][lt]);
        SB();
        if constexpr (i + 3 < 7) lda(std::integral_constant<int, i + 3>{}, lc);
        if constexpr (i == 4 && lt == 0) {
          xres = __builtin_amdgcn_raw_buffer_load_b128(rs_x, (64 * bp + (t >> 3)) * 1024 + bc * 128 + (t & 7) * 16, 0, 0);
          if (w >= 4) fy_v = probe(fy_p);
        }
      });
    });
    AWAIT(E_CONV);
#pragma unroll
    for (int dt = 0; dt < 4; ++dt) {
      const int sg = ((2 * dt + (h >> 1)) & 3) * 4;
      LAS char* pw = opq(S + dt * STAGE + 4096 * w + h * 256 + ((l15 ^ sg) * 16));
#pragma unroll
      for (int lt = 0; lt < 4; ++lt) *(LAS f32x4*)(pw + lt * 1024) = ac[dt][lt];
    }
    if (w >= 4) {
      wait_flags(fy_p, fy_v, tag);
      const int yc = 16 * q4 + (l >> 3);
      const u32 yoff = WS_YT + ((64 * bc + yc) * 2048 + 64 * bp + (l & 7) * 8) * 2;
      u32x4 y0 = __builtin_amdgcn_raw_buffer_load_b128(rs_ws, yoff, 0, 16);
      u32x4 y1 = __builtin_amdgcn_raw_buffer_load_b128(rs_ws, yoff, 8 * 4096, 16);
      LAS char* yw = opq(S + YI + yc * 128 + (((l & 7) ^ (2 * q4)) * 16));
      *(LAS u32x4*)(yw) = y0;
      *(LAS u32x4*)((LAS char*)((size_t)yw ^ 16) + 1024) = y1;
    }
    BAR();
    {
      const int lrow = t >> 3, g = t & 7;
      LAS char* pr = opq(S + (g >> 1) * STAGE + (lrow >> 4) * 1024 + (2 * (g & 1)) * 256 + (((lrow & 15) ^ ((g & 3) * 4)) * 16));
      LAS char* yr = opq(S + YI + 8 * g * 128 + (((lrow >> 3) ^ g) * 16) + (lrow & 7) * 2);
      f32x4 s0 = *(LAS f32x4*)(pr), s1 = *(LAS f32x4*)(pr + 256);
#pragma unroll
      for (int p = 1; p < 8; ++p) {
        s0 += *(LAS f32x4*)(pr + 4096 * p);
        s1 += *(LAS f32x4*)(pr + 4096 * p + 256);
      }
      float yb[8] = {s0[0], s0[1], s0[2], s0[3], s1[0], s1[1], s1[2], s1[3]};
      u32 o[4];
#pragma unroll
      for (int q = 0; q < 4; ++q) {
        u32 y0 = *(LAS u16*)(yr + (2 * q) * 128);
        u32 y1 = *(LAS u16*)(yr + (2 * q + 1) * 128);
        u32 a = pkbf(bflo(xres[q]) + bflo(y0), bfhi(xres[q]) + bflo(y1));
        u32 bq = pkbf(yb[2 * q], yb[2 * q + 1]);
        o[q] = pkbf(bflo(a) + bflo(bq), bfhi(a) + bfhi(bq));
      }
      __builtin_amdgcn_raw_buffer_store_b128(u32x4{o[0], o[1], o[2], o[3]}, rs_out, (64 * bp + lrow) * 1024 + bc * 128 + g * 16, 0, 0);
    }
  }
  STAMP(11);
}

#if defined(__HIP_DEVICE_COMPILE__)
#pragma clang attribute pop
#endif

extern "C" void kernel_launch(void* const* d_in, const int* in_sizes, int n_in, void* d_out, int out_size, void* d_ws,
                              size_t ws_size, hipStream_t stream) {
  Params P;
  P.x = (const u16*)d_in[0];
  P.Are = (const u16*)d_in[1];
  P.Aim = (const u16*)d_in[2];
  P.Bre = (const u16*)d_in[3];
  P.Bim = (const u16*)d_in[4];
  P.Cre = (const u16*)d_in[5];
  P.Cim = (const u16*)d_in[6];
  P.logdt = (const u16*)d_in[7];
  P.dskip = (const u16*)d_in[8];
  P.w1 = (const u16*)d_in[10];
  P.b1 = (const u16*)d_in[11];
  P.w2 = (const u16*)d_in[12];
  P.out = (u16*)d_out;
  P.ws = (char*)d_ws;
  (void)hipFuncSetAttribute((const void*)fused_kernel, hipFuncAttributeMaxDynamicSharedMemorySize, LDS_BYTES);
  fused_kernel<<<256, 512, LDS_BYTES, stream>>>(P);
}
```

```cpp
#if defined(__HIP_DEVICE_COMPILE__)
#pragma clang attribute push(__attribute__((target("no-packed-fp32-ops"))), apply_to = function)
#endif
#include <hip/hip_runtime.h>
#include <stdint.h>
#include <type_traits>

typedef unsigned short u16;
typedef unsigned int u32;
typedef unsigned long long u64;
typedef short s16x4 __attribute__((ext_vector_type(4)));
typedef _Float16 h16x2 __attribute__((ext_vector_type(2)));
typedef _Float16 h16x8 __attribute__((ext_vector_type(8)));
typedef __bf16 b16x2 __attribute__((ext_vector_type(2)));
typedef __bf16 b16x8 __attribute__((ext_vector_type(8)));
typedef float f32x2 __attribute__((ext_vector_type(2)));
typedef float f32x4 __attribute__((ext_vector_type(4)));
typedef u32 u32x2 __attribute__((ext_vector_type(2)));
typedef u32 u32x4 __attribute__((ext_vector_type(4)));
typedef int i32x4 __attribute__((ext_vector_type(4)));
#define LAS __attribute__((address_space(3)))
#define GAS __attribute__((address_space(1)))
#define RLX_AGENT __ATOMIC_RELAXED, __HIP_MEMORY_SCOPE_AGENT

constexpr int L_ = 2048, D_ = 512, N_ = 128, F_ = 1792;
#define S_PRIO 0
#define POLL_SLEEP 8
#define M_PRIO_E1 2
#define S2_BY_S 1
#define CODE_PF_BYTES 24576

constexpr u32 WS_STAT = 4096;
constexpr u32 FSTR = 128;
constexpr u32 WS_FXN = 32768;
constexpr u32 WS_FACT = WS_FXN + 1024 * FSTR;
constexpr u32 WS_FY = WS_FACT + 2048 * FSTR;
constexpr u32 WS_XNT = 1048576;
constexpr u32 WS_ACT = WS_XNT + 2097152;
constexpr u32 WS_YT = WS_ACT + 7340032;
constexpr u32 WS_END = WS_YT + 2097152;

constexpr u32 STAGE = 32768;
constexpr u32 KT = 131072;
constexpr u32 KTW = 4352;
constexpr u32 XT = 148480;
constexpr u32 KX = 148480;
constexpr u32 KRO = 148480;
constexpr u32 KRS = 4576;
constexpr u32 DUM = 157632;
constexpr u32 SYN = 158656;
constexpr u32 CSO = 158784;
constexpr u32 STO = 129024;
constexpr u32 B1O = 129536;
constexpr u32 KLIN = 161248;
constexpr u32 KLS = 1296;
constexpr u32 UC = 131072;
constexpr u32 UCS = 4160;
constexpr u32 CRED = 131584;
constexpr u32 YI = 131584;
constexpr u32 LDS_BYTES = 163840;
enum { C_LANDED = 0, E_XT = 16, E_XTR, E_XNST, E_KX, E_KLIN, E_UC, E_CONV, E_YST, E_CS, E_ACT, E_CRED, E_W2 };

constexpr int SPIN_CAP = 1 << 16;

__device__ __forceinline__ float bflo(u32 v) { return __uint_as_float(v << 16); }
__device__ __forceinline__ float bfhi(u32 v) { return __uint_as_float(v & 0xffff0000u); }
__device__ __forceinline__ u32 pkbf(float a, float b) {
  f32x2 v = {a, b};
  return __builtin_bit_cast(u32, __builtin_convertvector(v, b16x2));
}
__device__ __forceinline__ u32 pkh(float a, float b) {
  f32x2 v = {a, b};
  return __builtin_bit_cast(u32, __builtin_convertvector(v, h16x2));
}
__device__ __forceinline__ float rbf(float a) { return bflo(pkbf(a, 0.f)); }

__device__ __forceinline__ i32x4 mk_srd(const void* p, u32 bytes) {
  u64 a = (u64)p;
  i32x4 r;
  r[0] = (int)(u32)a;
  r[1] = (int)((u32)(a >> 32) & 0xffffu);
  r[2] = (int)bytes;
  r[3] = 0x00020000;
  return r;
}
__device__ __forceinline__ void dma16(i32x4 srd, u32 voff, u32 soff, u32 dst) {
  asm volatile("s_mov_b32 m0, %3\n\ts_nop 0\n\tbuffer_load_dwordx4 %0, %1, %2 offen lds"
               :: "v"(voff), "s"(srd), "s"(__builtin_amdgcn_readfirstlane(soff)), "s"(__builtin_amdgcn_readfirstlane(dst)));
}
__device__ __forceinline__ void dma4(i32x4 srd, u32 voff, u32 dst) {
  asm volatile("s_mov_b32 m0, %2\n\ts_nop 0\n\tbuffer_load_dword %0, %1, 0 offen lds"
               :: "v"(voff), "s"(srd), "s"(__builtin_amdgcn_readfirstlane(dst)));
}
__device__ __forceinline__ u32 ald4(i32x4 srd, u32 voff) {
  u32 r;
  asm volatile("buffer_load_dword %0, %1, %2, 0 offen" : "=v"(r) : "v"(voff), "s"(srd) : "memory");
  return r;
}
__device__ __forceinline__ u32x2 ald8(i32x4 srd, u32 voff) {
  u32x2 r;
  asm volatile("buffer_load_dwordx2 %0, %1, %2, 0 offen" : "=v"(r) : "v"(voff), "s"(srd) : "memory");
  return r;
}
__device__ __forceinline__ u32x4 ald16(i32x4 srd, u32 voff) {
  u32x4 r;
  asm volatile("buffer_load_dwordx4 %0, %1, %2, 0 offen" : "=v"(r) : "v"(voff), "s"(srd) : "memory");
  return r;
}
#define BAR() asm volatile("s_waitcnt lgkmcnt(0)\n\ts_barrier" ::: "memory")
#define WAITV(n) asm volatile("s_waitcnt vmcnt(" #n ")" ::: "memory")
#define SB() __builtin_amdgcn_sched_barrier(0)

__device__ __forceinline__ f32x4 mfma_bf(u32x4 a, u32x4 b, f32x4 c) {
  return __builtin_amdgcn_mfma_f32_16x16x32_bf16(__builtin_bit_cast(b16x8, a), __builtin_bit_cast(b16x8, b), c, 0, 0, 0);
}
__device__ __forceinline__ f32x4 mfma_h(u32x4 a, u32x4 b, f32x4 c) {
  return __builtin_amdgcn_mfma_f32_16x16x32_f16(__builtin_bit_cast(h16x8, a), __builtin_bit_cast(h16x8, b), c, 0, 0, 0);
}
__device__ __forceinline__ u32x2 tr_rd(LAS char* p) {
  return __builtin_bit_cast(u32x2, __builtin_amdgcn_ds_read_tr16_b64_v4i16((LAS s16x4*)p));
}
__device__ __forceinline__ u32x4 cat(u32x2 a, u32x2 b) { return u32x4{a[0], a[1], b[0], b[1]}; }
template <int CTRL> __device__ __forceinline__ float dpp_mov(float v) {
  return __builtin_bit_cast(float, __builtin_amdgcn_update_dpp(0, __builtin_bit_cast(int, v), CTRL, 0xf, 0xf, true));
}
__device__ __forceinline__ float rdlane(float v, int i) {
  return __builtin_bit_cast(float, __builtin_amdgcn_readlane(__builtin_bit_cast(int, v), i));
}
__device__ __forceinline__ float wave_max(float v) {
  v = fmaxf(v, dpp_mov<0xB1>(v));
  v = fmaxf(v, dpp_mov<0x4E>(v));
  v = fmaxf(v, dpp_mov<0x141>(v));
  v = fmaxf(v, dpp_mov<0x140>(v));
  return fmaxf(fmaxf(rdlane(v, 0), rdlane(v, 16)), fmaxf(rdlane(v, 32), rdlane(v, 48)));
}
__device__ __forceinline__ float wave_sum(float v) {
  v += dpp_mov<0xB1>(v);
  v += dpp_mov<0x4E>(v);
  v += dpp_mov<0x141>(v);
  v += dpp_mov<0x140>(v);
  return (rdlane(v, 0) + rdlane(v, 16)) + (rdlane(v, 32) + rdlane(v, 48));
}
__device__ __forceinline__ u64 probe(GAS u64* p) { return __hip_atomic_load(p, RLX_AGENT); }
__device__ __forceinline__ void wait_flags(GAS u64* p, u64 v, u64 tag) {
  if (!__all(v == tag)) {
#pragma unroll 1
    for (int it = 0; it < SPIN_CAP; ++it) {
      __builtin_amdgcn_s_sleep(POLL_SLEEP);
      v = __hip_atomic_load(p, RLX_AGENT);
      if (__all(v == tag)) break;
    }
  }
  __builtin_amdgcn_fence(__ATOMIC_ACQUIRE, "wavefront");
}
template <int I, int N, class F> __device__ __forceinline__ void static_for(F&& f) {
  if constexpr (I < N) { f(std::integral_constant<int, I>{}); static_for<I + 1, N>(f); }
}
#define CV(x) decltype(x)::value
__device__ __forceinline__ void lds_arrive(u32 addr) {
  u32 one = 1;
  asm volatile("s_mov_b64 exec, 1\n\tds_add_u32 %0, %1\n\ts_mov_b64 exec, -1" :: "v"(addr), "v"(one) : "memory");
}
__device__ __forceinline__ void lds_wait(LAS char* p, u32 target) {
#pragma unroll 1
  for (int it = 0; it < (1 << 22); ++it) {
    u32 v = *(volatile LAS u32*)p;
    if ((u32)__builtin_amdgcn_readfirstlane(v) >= target) break;
    __builtin_amdgcn_s_sleep(1);
  }
  asm volatile("" ::: "memory");
}
__device__ __forceinline__ void lds_spin(LAS char* p, u32 target) {
#pragma unroll 1
  for (int it = 0; it < (1 << 24); ++it) {
    u32 v = *(volatile LAS u32*)p;
    if ((u32)__builtin_amdgcn_readfirstlane(v) >= target) break;
  }
  asm volatile("" ::: "memory");
}
__device__ __forceinline__ LAS char* opq(LAS char* p) { asm volatile("" : "+v"(p)); return p; }
__device__ __forceinline__ float half_sum(float v) {
  v += dpp_mov<0xB1>(v);
  v += dpp_mov<0x4E>(v);
  v += dpp_mov<0x141>(v);
  v += dpp_mov<0x140>(v);
  auto r = __builtin_amdgcn_permlane16_swap(__builtin_bit_cast(u32, v), __builtin_bit_cast(u32, v), false, false);
  return __builtin_bit_cast(float, (u32)r[0]) + __builtin_bit_cast(float, (u32)r[1]);
}
struct cpx { float re, im; };
__device__ __forceinline__ cpx cmul(cpx a, cpx b) {
  cpx r;
  r.re = fmaf(a.re, b.re, -(a.im * b.im));
  r.im = fmaf(a.re, b.im, a.im * b.re);
  return r;
}

struct Params {
  const u16 *x, *Are, *Aim, *Bre, *Bim, *Cre, *Cim, *logdt, *dskip, *w1, *b1, *w2;
  u16* out;
  char* ws;
};

__global__ void __launch_bounds__(512, 2) fused_kernel(Params P) {
  extern __shared__ __attribute__((aligned(16))) char smem[];
  LAS char* S = (LAS char*)smem;
  const u32 sbase = (u32)(size_t)S;
  char* ws = P.ws;
  const int t = __builtin_amdgcn_workitem_id_x(), l = t & 63, w = __builtin_amdgcn_readfirstlane(t >> 6);
  const int h = l >> 4, l15 = l & 15;
  const int b = __builtin_amdgcn_workgroup_id_x(), xcd = b & 7, rk = b >> 3;
  const int bi = 4 * (xcd >> 1) + (rk >> 3), bj = 8 * (xcd & 1) + (rk & 7);
  const int bp = 4 * xcd + (rk >> 3), bc = rk & 7;
  const int q4 = w & 3;
  if (w == 7) dma16(mk_srd((const void*)__builtin_amdgcn_s_getpc(), CODE_PF_BYTES), rk * 1024 + l * 16, 0, sbase + DUM);

  const __amdgpu_buffer_rsrc_t rs_ws = __builtin_amdgcn_make_buffer_rsrc((void*)ws, 0, WS_END, 0x00020000);
  const __amdgpu_buffer_rsrc_t rs_x = __builtin_amdgcn_make_buffer_rsrc((void*)P.x, 0, L_ * D_ * 2, 0x00020000);
  const __amdgpu_buffer_rsrc_t rs_out = __builtin_amdgcn_make_buffer_rsrc((void*)P.out, 0, L_ * D_ * 2, 0x00020000);
  const i32x4 sx = mk_srd(P.x, L_ * D_ * 2), sw1 = mk_srd(P.w1, D_ * F_ * 2), sw2 = mk_srd(P.w2, D_ * F_ * 2);
  u64 tag;
  u32 ldt2, dsk2;
  asm volatile("s_load_dwordx2 %0, %3, 0x0\n\ts_load_dword %1, %4, 0x0\n\ts_load_dword %2, %5, 0x0"
               : "=&s"(tag), "=&s"(ldt2), "=&s"(dsk2) : "s"(ws), "s"(P.logdt + 2 * b), "s"(P.dskip + 2 * b));

  u32 p_ar, p_ai, p_br, p_bi, p_cr, p_ci;
  u32x4 sr0, sr1;
  const int cc = q4 >> 1, hm = q4 & 1, ch = 2 * b + cc;
  if (w < 4) {
    if (t < 32) *(LAS u32*)(S + SYN + t * 4) = 0;
  } else {
    const u32 o = (128 * bi + 8 * bj + 2 * q4) * 1024 + l * 32;
    sr0 = ald16(sx, o);
    sr1 = ald16(sx, o + 16);
    const u32 po = ch * 256 + 4 * l;
    p_ar = ald4(mk_srd(P.Are, D_ * N_ * 2), po);
    p_ai = ald4(mk_srd(P.Aim, D_ * N_ * 2), po);
    p_br = ald4(mk_srd(P.Bre, D_ * N_ * 2), po);
    p_bi = ald4(mk_srd(P.Bim, D_ * N_ * 2), po);
    p_cr = ald4(mk_srd(P.Cre, D_ * N_ * 2), po);
    p_ci = ald4(mk_srd(P.Cim, D_ * N_ * 2), po);
  }
  const u32 dq = q4 < 3 ? 4096u : 12288u, dq2 = q4 < 3 ? 11264u : 12288u;
  const u32 vx = (128 * bi + 8 * q4 + (l >> 3)) * 1024 + (((l & 7) ^ (l >> 3)) * 16);
  u32 vwa, vwb;
  {
    const u32 g0 = q4 * 64 + l, pos0 = (g0 * 4682u) >> 16, c0 = g0 - pos0 * 14;
    const u32 g1 = g0 + 256, pos1 = (g1 * 4682u) >> 16, c1 = g1 - pos1 * 14;
    const u32 k0 = (pos0 & ~12u) | ((pos0 & 4) << 1) | ((pos0 & 8) >> 1);
    const u32 k1 = (pos1 & ~12u) | ((pos1 & 4) << 1) | ((pos1 & 8) >> 1);
    vwa = k0 * (F_ * 2) + bj * 224 + c0 * 16;
    vwb = q4 < 3 ? k1 * (F_ * 2) + bj * 224 + c1 * 16 : 0x7ff00000u;
  }
  auto issue_stage = [&](int c) {
    const u32 sb_ = sbase + c * STAGE + q4 * 1024;
#pragma unroll
    for (int i = 0; i < 4; ++i) dma16(sx, vx, c * 128 + i * 32768, sb_ + i * 4096);
    const u32 so = c * (64 * F_ * 2);
    dma16(sw1, vwa, so, sb_ + 16384);
    dma16(sw1, vwa, so + 32 * F_ * 2, sb_ + 16384 + 7168);
    dma16(sw1, vwb, so, sb_ + 16384 + dq);
    dma16(sw1, vwb, so + 32 * F_ * 2, sb_ + 16384 + dq2);
  };
  if (w < 4) issue_stage(0);
  BAR();
  asm volatile("" : "+s"(tag), "+s"(ldt2), "+s"(dsk2));
  tag += 1ull;
  issue_stage(1 + (w >> 2));
  u32 w2o;
  LAS char* w2a[4];
  {
    const int rr = l >> 3, slot = l & 7;
    const int dt = (slot >> 1) ^ ((rr >> 1) & 3);
    w2o = (224 * w + rr) * 1024 + bc * 128 + (2 * dt + (slot & 1)) * 16;
    const int q = l15 >> 2, p = l & 3, x = (2 * h + (q >> 1)) & 3;
#pragma unroll
    for (int d = 0; d < 4; ++d) w2a[d] = S + 4096 * w + (4 * h + q) * 128 + ((d ^ x) * 32) + (p >> 1) * 16 + (p & 1) * 8;
  }
#define W2TILE(ks, hf) ((ks) < 3 ? 2 * (ks) + (hf) : ((ks) == 3 ? ((hf) ? 13 : 6) : 7 + 2 * ((ks) - 4) + (hf)))
#define ISSUE_W2(p, n, slot) dma16(sw2, w2o, (W2TILE(p, (n) >> 1) * 16 + ((n) & 1) * 8) * 1024, sbase + (slot) * STAGE + 4096 * w + (n) * 1024)
  auto w2_frag = [&](int d, int slot) {
    LAS char* p = slot >= 2 ? opq(w2a[d] + 2 * STAGE) + (slot - 2) * STAGE : w2a[d] + slot * STAGE;
    return cat(tr_rd(p), tr_rd(p + 2048));
  };
  u32x4 wr1[4][4];
  auto w2_regs = [&]() {
#pragma unroll
    for (int k = 0; k < 4; ++k)
#pragma unroll
      for (int d = 0; d < 4; ++d) wr1[k][d] = w2_frag(d, k);
    asm volatile("s_waitcnt lgkmcnt(0)" ::: "memory");
  };
  auto w2_req = [&]() {
#pragma unroll
    for (int n = 0; n < 4; ++n) ISSUE_W2(2, n, 0);
#pragma unroll
    for (int n = 0; n < 4; ++n) ISSUE_W2(3, n, 1);
#pragma unroll
    for (int n = 0; n < 4; ++n) ISSUE_W2(6, n, 2);
  };
  f32x4 ca[4];
  const u32 syn = sbase + SYN;
  LAS char* synp = opq(S + SYN);
#define ARRIVE(e) lds_arrive(syn + (e) * 4)
#define AWAIT(e) lds_wait(synp + (e) * 4, 4)

  if (w < 4) {
    f32x4 acc[7][2], accs[7];
#pragma unroll
    for (int f = 0; f < 7; ++f) { acc[f][0] = f32x4{0, 0, 0, 0}; acc[f][1] = f32x4{0, 0, 0, 0}; accs[f] = f32x4{0, 0, 0, 0}; }
    u32 xa = (32 * q4 + l15) * 128 + ((h ^ (l & 7)) * 16);
    u32 wo = 16384 + (16 * (h >> 1) + 4 * (h & 1) + (l15 >> 2)) * 224 + (l & 3) * 8;
    u32x4 ones = {0x3f803f80u, 0x3f803f80u, 0x3f803f80u, 0x3f803f80u};
    asm volatile("" : "+v"(ones));
    u32x4 xf[2][2];
    u32x2 wl[2][7], wh[2][7];
    u32 lnd = 0;
    auto kstep = [&](auto kc, auto mc, u32 so, u32 dst, u32 cnt_addr, LAS char* cnt_p, u32 tgt, int nwait) {
      constexpr int k = CV(kc), n = k ^ 1, MODE = CV(mc);
      constexpr int r0 = k ? 4 : 0;
      LAS char* xp = S + (k ? xa : (xa ^ 64));
      LAS char* wp = S + wo + (k ? 0 : 32 * 224);
      static_for<0, 14>([&](auto jc) {
        constexpr int j = CV(jc), f = j >> 1, mt = j & 1;
        SB();
        acc[f][mt] = mfma_bf(cat(wl[k][f], wh[k][f]), xf[k][mt], acc[f][mt]);
        SB();
        if constexpr (k == 1 && j == 1) lnd = *(volatile LAS u32*)cnt_p;
        if constexpr (k == 1 && j == 3) { if ((u32)__builtin_amdgcn_readfirstlane(lnd) < tgt) lds_spin(cnt_p, tgt); }
        if constexpr (j == r0) { xf[n][0] = *(LAS u32x4*)(xp); xf[n][1] = *(LAS u32x4*)(xp + 2048); }
        if constexpr (j >= r0 + 1 && j <= r0 + 7) {
          wl[n][j - r0 - 1] = tr_rd(wp + (j - r0 - 1) * 32);
          wh[n][j - r0 - 1] = tr_rd(wp + 8 * 224 + (j - r0 - 1) * 32);
        }
        constexpr int pc = j == (k ? 5 : 1) ? 0 : j == (k ? 8 : 5) ? 1 : j == (k ? 11 : 10) ? 2 : j == (k ? 13 : 12) ? 3 : -1;
        if constexpr (pc >= 0 && MODE == 1 && k == 0)
          dma16(sw1, pc < 2 ? vwa : vwb, so + (pc & 1) * (32 * F_ * 2), dst + (pc == 0 ? 0u : pc == 1 ? 7168u : pc == 2 ? dq : dq2));
        if constexpr (pc >= 0 && MODE == 1 && k == 1) dma16(sx, vx, so + pc * 32768, dst + pc * 4096);
        if constexpr (pc >= 0 && MODE == 2) dma16(sw2, w2o, so + pc * 8192, dst + pc * 1024);
        if constexpr (k == 0 && j == 8) {
          if (nwait == 2) WAITV(2); else if (nwait) WAITV(10);
          lds_arrive(cnt_addr);
        }
      });
      SB();
    };
    auto colsum = [&](auto kc) {
#pragma unroll
      for (int f = 0; f < 7; ++f) accs[f] = mfma_bf(cat(wl[CV(kc)][f], wh[CV(kc)][f]), ones, accs[f]);
    };
    using I0 = std::integral_constant<int, 0>; using I1 = std::integral_constant<int, 1>; using I2 = std::integral_constant<int, 2>;
    WAITV(8);
    ARRIVE(C_LANDED);
    lds_spin(synp + C_LANDED * 4, 4);
    xf[0][0] = *(LAS u32x4*)(S + xa); xf[0][1] = *(LAS u32x4*)(S + xa + 2048);
#pragma unroll
    for (int f = 0; f < 7; ++f) { wl[0][f] = tr_rd(S + wo + f * 32); wh[0][f] = tr_rd(S + wo + 8 * 224 + f * 32); }
    auto adv = [&]() { xa = (xa + STAGE) & 131071u; wo = (wo + STAGE) & 131071u; };
#pragma unroll 1
    for (int c = 0; c < 5; ++c) {
      const u32 sl = sbase + ((c + 3) & 3) * STAGE + q4 * 1024;
      kstep(I0{}, I1{}, (c + 3) * (64 * F_ * 2), sl + 16384, syn + (c + 1) * 4, synp, 0, (S2_BY_S && c == 0) ? 2 : 10);
      if (((2 * c) & 3) == q4) colsum(I0{});
      adv();
      kstep(I1{}, I1{}, (c + 3) * 128, sl, 0, synp + (c + 1) * 4, (S2_BY_S && c == 1) ? 8u : 4u, 0);
      if (((2 * c + 1) & 3) == q4) colsum(I1{});
    }
#pragma unroll 1
    for (int c = 5; c < 8; ++c) {
      const u32 sl = sbase + (c - 5) * STAGE + 4096 * w, wso = c < 7 ? (c - 5) * 32768 : 7 * 16384;
      kstep(I0{}, I2{}, wso, sl, syn + (c + 1) * 4, synp, 0, c < 7);
      if (((2 * c) & 3) == q4) colsum(I0{});
      adv();
      kstep(I1{}, I2{}, wso + 896 * 1024, sl + 16384, 0, synp + (c + 1) * 4, c < 7 ? 4u : 0u, 0);
      if (((2 * c + 1) & 3) == q4) colsum(I1{});
    }
    if (l15 == 0) {
      LAS char* cp = opq(S + CSO + q4 * 448 + h * 16);
#pragma unroll
      for (int f = 0; f < 7; ++f) *(LAS f32x4*)(cp + 64 * f) = accs[f];
    }
    AWAIT(E_XT);
    u32 pay2[2];
    u32x2 b1v[7];
    {
      LAS char* sp = opq(S + STO + (32 * q4 + l15) * 4);
      LAS char* bp_ = opq(S + B1O + h * 8);
      pay2[0] = *(LAS u32*)sp; pay2[1] = *(LAS u32*)(sp + 64);
#pragma unroll
      for (int f = 0; f < 7; ++f) b1v[f] = *(LAS u32x2*)(bp_ + 32 * f);
    }
    ARRIVE(E_CS);
    const float LOG2E = 1.4426950408889634f, LN2 = 0.6931471805599453f;
    float rs[2], tm[2];
#pragma unroll
    for (int mt = 0; mt < 2; ++mt) {
      rs[mt] = __builtin_amdgcn_rcpf(bfhi(pay2[mt])) * LOG2E;
      tm[mt] = -bflo(pay2[mt]) * rs[mt];
    }
    lds_spin(synp + E_CS * 4, 4);
    __builtin_amdgcn_s_setprio(M_PRIO_E1);
    const int jp = bj & 1;
    const u32 abase = WS_ACT + ((8 * bi + 2 * q4) * 56 + 7 * (bj >> 1) + (jp ? 4 : 0)) * 1024 + l * 16;
    LAS char* csp = opq(S + CSO + h * 16);
    u32x2 pk[2][2];
    static_for<0, 7>([&](auto fc) {
      constexpr int f = CV(fc);
      const f32x4 cs = (*(LAS f32x4*)(csp + 64 * f) + *(LAS f32x4*)(csp + 64 * f + 448)) + (*(LAS f32x4*)(csp + 64 * f + 896) + *(LAS f32x4*)(csp + 64 * f + 1344));
      const float bia[4] = {bflo(b1v[f][0]) * LOG2E, bfhi(b1v[f][0]) * LOG2E, bflo(b1v[f][1]) * LOG2E, bfhi(b1v[f][1]) * LOG2E};
#pragma unroll
      for (int mt = 0; mt < 2; ++mt) {
        float a[4];
#pragma unroll
        for (int r = 0; r < 4; ++r) {
          float g = fmaf(acc[f][mt][r], rs[mt], fmaf(tm[mt], cs[r], bia[r]));
          float s1 = __builtin_amdgcn_exp2f(g) + 1.f;
          float rr = __builtin_amdgcn_rcpf(fmaf(s1, s1, 1.f));
          a[r] = g * fmaf(rr, -2.f * LN2, LN2);
        }
        pk[f & 1][mt] = u32x2{pkbf(a[0], a[1]), pkbf(a[2], a[3])};
        if constexpr (f < 2) {
          SB();
          const int n = 2 * f + mt;
          const u32 so = 9 * 16384 + n * 8192, dd = sbase + 3 * STAGE + 4096 * w + n * 1024;
          dma16(sw2, w2o, so, dd); dma16(sw2, w2o, so + 896 * 1024, dd + 16384);
          SB();
        }
      }
      if constexpr (f & 1) {
#pragma unroll
        for (int mt = 0; mt < 2; ++mt)
          __builtin_amdgcn_raw_buffer_store_b128(cat(pk[0][mt], pk[1][mt]), rs_ws, abase + (f >> 1) * 1024, mt * 56 * 1024, 16);
      }
      if constexpr (f == 6) {
#pragma unroll
        for (int mt = 0; mt < 2; ++mt)
          __builtin_amdgcn_raw_buffer_store_b64(pk[0][mt], rs_ws, abase + (jp ? -1024 + 8 : 3072), mt * 56 * 1024, 16);
      }
    });
    WAITV(6);
    ARRIVE(E_W2);
    w2_regs();
    WAITV(0);
    if (l < 2) __hip_atomic_store((GAS u64*)(ws + WS_FACT + ((bi * 16 + bj) * 8 + 4 * l + q4) * FSTR), tag, RLX_AGENT);
    __builtin_amdgcn_s_setprio(0);
    w2_req();
  } else {
    const int ts = t - 256;
    __builtin_amdgcn_s_setprio(S_PRIO);
    WAITV(8);
    asm volatile("" : "+v"(p_ar), "+v"(p_ai), "+v"(p_br), "+v"(p_bi), "+v"(p_cr), "+v"(p_ci));
    asm volatile("" : "+v"(sr0), "+v"(sr1));
    {
      float e[16];
#pragma unroll
      for (int q = 0; q < 4; ++q) { e[2 * q] = bflo(sr0[q]); e[2 * q + 1] = bfhi(sr0[q]); e[8 + 2 * q] = bflo(sr1[q]); e[9 + 2 * q] = bfhi(sr1[q]); }
      float s0 = (e[0] + e[1]) + (e[2] + e[3]), s1 = (e[4] + e[5]) + (e[6] + e[7]), s2 = (e[8] + e[9]) + (e[10] + e[11]), s3 = (e[12] + e[13]) + (e[14] + e[15]);
      const float mean = half_sum((s0 + s1) + (s2 + s3)) * (1.0f / D_);
      float v0 = 0, v1 = 0;
#pragma unroll
      for (int q = 0; q < 8; ++q) { float d0 = e[q] - mean, d1 = e[8 + q] - mean; v0 = fmaf(d0, d0, v0); v1 = fmaf(d1, d1, v1); }
      const float var = half_sum(v0 + v1) * (1.0f / D_);
      const float sd = __builtin_amdgcn_sqrtf(rbf(rbf(var) + 1e-5f));
      const u32 pay = pkbf(mean, sd);
      if ((l & 31) == 0) __hip_atomic_store((GAS u64*)(ws + WS_STAT + (128 * bi + 8 * bj + 2 * q4 + (l >> 5)) * 8), ((u64)(u32)tag << 32) | pay, RLX_AGENT);
    }
    WAITV(1);
    ARRIVE(C_LANDED + 2);
    u32x4 xs0 = ald16(sx, (128 * bi + (ts >> 1)) * 1024 + bj * 64 + (ts & 1) * 32);
    u32x4 xs1 = ald16(sx, (128 * bi + (ts >> 1)) * 1024 + bj * 64 + (ts & 1) * 32 + 16);
    u32 b1w = ald4(mk_srd(P.b1, F_ * 2), bj * 224 + 4 * l);
    float yscale, gmax, zr, zi;
    cpx cbn;
    {
      const float LOG2E = 1.4426950408889634f, INV2PI = 0.15915494309189535f;
      const float lx = cc ? bfhi(ldt2) : bflo(ldt2);
      const float th = lx * LOG2E, tl = fmaf(lx, LOG2E, -th) + lx * 1.925963033500011e-8f;
      const float dt = rbf(__builtin_amdgcn_exp2f(th) * fmaf(tl, 0.6931471805599453f, 1.0f));
      cpx cb0 = cmul(cpx{bflo(p_cr), bflo(p_ci)}, cpx{dt * bflo(p_br), dt * bflo(p_bi)});
      cpx cb1 = cmul(cpx{bfhi(p_cr), bfhi(p_ci)}, cpx{dt * bfhi(p_br), dt * bfhi(p_bi)});
      const float z0 = dt * bflo(p_ar) * LOG2E, z1 = dt * bfhi(p_ar) * LOG2E;
      gmax = fmaxf(wave_max(fmaxf(z0, z1)), 0.f);
      const float cbmax = wave_max(fmaxf(fmaxf(fabsf(cb0.re), fabsf(cb0.im)), fmaxf(fabsf(cb1.re), fabsf(cb1.im))));
      const float cbs = __uint_as_float((__float_as_uint(cbmax) + 0x007fffffu) & 0x7f800000u);
      const float icb = cbs > 0.f ? __builtin_amdgcn_rcpf(cbs) : 0.f;
      yscale = cbs * __builtin_amdgcn_exp2f(gmax * 2048.f) * (1.0f / 64.f);
      zr = fmaxf((hm ? z1 : z0) - gmax, -1e30f);
      zi = dt * (hm ? bfhi(p_ai) : bflo(p_ai)) * INV2PI;
      cbn = hm ? cb1 : cb0;
      cbn.re *= 64.f * icb; cbn.im *= -64.f * icb;
    }
    auto cexp = [&](float a) {
      float mg = __builtin_amdgcn_exp2f(zr * a);
      float ph = __builtin_amdgcn_fractf(zi * a);
      return cpx{mg * __builtin_amdgcn_cosf(ph), mg * __builtin_amdgcn_sinf(ph)};
    };
    const cpx e1 = cexp(1.f), e8 = cexp(8.f), e128 = cexp(128.f);
    GAS u64* stat_p = (GAS u64*)(ws + WS_STAT + (128 * bi + (ts >> 1)) * 8);
    u64 stat_v = 0;
    LAS char* tb = opq(S + KT + q4 * KTW + l * 4);
    LAS char* tf = opq(S + KT + q4 * KTW + l15 * 272 + h * 16);
    auto put = [&](int row, cpx v) { *(volatile LAS u32*)(tb + row * 272) = pkh(v.re, v.im); };
    u32x4 fa[4];
    f32x4 kacc[3];
    auto tile_mma = [&]() {
      f32x4 a = {0, 0, 0, 0};
#pragma unroll
      for (int k = 0; k < 4; ++k) a = mfma_h(fa[k], *(volatile LAS u32x4*)(tf + k * 64), a);
      return a;
    };
    {
      cpx wv = cbn;
      const cpx e8c = cpx{e8.re, -e8.im};
#pragma unroll
      for (int q = 0; q < 16; ++q) { put(q, wv); if (q < 15) wv = cmul(wv, e8c); if (q == 6) stat_v = probe(stat_p); }
#pragma unroll
      for (int k = 0; k < 4; ++k) fa[k] = *(volatile LAS u32x4*)(tf + k * 64);
    }
    if (!__all((u32)(stat_v >> 32) == (u32)tag)) {
#pragma unroll 1
      for (int it = 0; it < SPIN_CAP; ++it) {
        __builtin_amdgcn_s_sleep(16);
        stat_v = probe(stat_p);
        if (__all((u32)(stat_v >> 32) == (u32)tag)) break;
      }
    }
    WAITV(0);
    asm volatile("" : "+v"(xs0), "+v"(xs1), "+v"(b1w));
    {
      const u32 pay = (u32)stat_v;
      const float mb = bflo(pay), rsd = __builtin_amdgcn_rcpf(bfhi(pay));
      *(LAS u32*)(S + STO + (ts >> 1) * 4) = pay;
      *(LAS u32*)(S + B1O + l * 4) = b1w;
      LAS char* xt = opq(S + XT + (ts & 1) * 16 * 272 + (ts >> 1) * 2);
#pragma unroll
      for (int q = 0; q < 8; ++q) {
        const u32 xv = q < 4 ? xs0[q] : xs1[q - 4];
        u32 dd = pkbf(bflo(xv) - mb, bfhi(xv) - mb);
        u32 xn = pkbf(bflo(dd) * rsd, bfhi(dd) * rsd);
        *(LAS u16*)(xt + (2 * q) * 272) = (u16)xn;
        *(LAS u16*)(xt + (2 * q + 1) * 272) = (u16)(xn >> 16);
      }
      ARRIVE(E_XT);
    }
    cpx e2, e4, e6;
    {
      cpx lo = cpx{1.f, 0.f}, g = e128;
#pragma unroll
      for (int q = 0; q < 8; ++q) {
        put(q, lo); put(8 + q, g);
        if (q == 2) e2 = lo;
        if (q == 4) e4 = lo;
        if (q == 6) e6 = lo;
        if (q < 7) { lo = (q == 0) ? e1 : cmul(lo, e1); g = cmul(g, e1); }
        if (q == 1) {
          AWAIT(E_XT);
          LAS char* xr = opq(S + XT + (ts >> 4) * 272 + (ts & 15) * 16);
          u32x4 v0 = *(LAS u32x4*)(xr), v1 = *(LAS u32x4*)(xr + 16 * 272);
          const u32 xo = WS_XNT + ((32 * bj + (ts >> 4)) * 2048 + 128 * bi + (ts & 15) * 8) * 2;
          __builtin_amdgcn_raw_buffer_store_b128(v0, rs_ws, xo, 0, 16);
          __builtin_amdgcn_raw_buffer_store_b128(v1, rs_ws, xo, 16 * 4096, 16);
          ARRIVE(E_XTR);
        }
      }
      kacc[0] = tile_mma();
    }
    cpx hv = cmul(e128, e128);
#pragma unroll
    for (int a = 2; a < 8; ++a) {
      if (a < 4) {
        put((a - 2) * 4 + 0, hv); put((a - 2) * 4 + 1, cmul(hv, e2));
        put((a - 2) * 4 + 2, cmul(hv, e4)); put((a - 2) * 4 + 3, cmul(hv, e6));
      } else {
        put(8 + (a - 4) * 2 + 0, hv); put(8 + (a - 4) * 2 + 1, cmul(hv, e4));
      }
      hv = cmul(hv, e128);
      if (a == 3) {
        WAITV(0);
        if (l == 0) __hip_atomic_store((GAS u64*)(ws + WS_FXN + ((bj * 16 + bi) * 4 + q4) * FSTR), tag, RLX_AGENT);
      }
    }
    kacc[1] = tile_mma();
#pragma unroll
    for (int a = 8; a <= 16; ++a) { put(a - 8, hv); if (a < 16) hv = cmul(hv, e128); }
    GAS u64* fxn_p = (GAS u64*)(ws + WS_FXN + (((ch >> 5) * 16 + l15) * 4 + ((ch & 15) >> 2)) * FSTR);
    u64 fxn_v;
    kacc[2] = tile_mma();
    AWAIT(E_XTR);
    {
      LAS char* kx = opq(S + KX + q4 * 2048 + l * 16);
      *(LAS f32x4*)(kx) = hm ? kacc[0] : kacc[1];
      *(LAS f32x4*)(kx + 1024) = kacc[2];
      ARRIVE(E_KX);
      fxn_v = probe(fxn_p);
      const int e = hm ? 1 + (l15 >> 3) : 0, lg = 3 - e, r8 = hm ? (l15 & 7) : l15;
      const int idx = (e ? 128 + 128 * e : 0) + ((r8 >> lg) << (7 - e)) + (r8 & ((1 << lg) - 1)) + ((4 * h) << lg);
      const int id2 = min(512 + 16 * l15 + 4 * h + 2 * hm, 642);
      LAS char* kl = S + KLIN + cc * KLS;
      AWAIT(E_KX);
      LAS char* ko_ = opq(S + KX + (q4 ^ 1) * 2048 + l * 16);
      f32x4 ka = (hm ? kacc[1] : kacc[0]) + *(LAS f32x4*)(ko_);
      f32x4 kb = kacc[2] + *(LAS f32x4*)(ko_ + 1024);
      float k2a = hm ? kb[2] : kb[0], k2b = hm ? kb[3] : kb[1];
      if (gmax > 0.f) {
#pragma unroll
        for (int r = 0; r < 4; ++r) ka[r] *= __builtin_amdgcn_exp2f(gmax * (float)(((idx + (r << lg) - 128 * e) << e) - 2048));
        k2a *= __builtin_amdgcn_exp2f(gmax * (float)(((id2 - 384) << 3) - 2048));
        k2b *= __builtin_amdgcn_exp2f(gmax * (float)(((id2 - 383) << 3) - 2048));
      }
#pragma unroll
      for (int r = 0; r < 4; ++r) *(LAS _Float16*)(kl + (idx + (r << lg)) * 2) = (_Float16)ka[r];
      *(LAS u32*)(kl + id2 * 2) = pkh(k2a, k2b);
    }
    ARRIVE(E_KLIN);
    const int tc = hm * 64 + l;
    wait_flags(fxn_p, fxn_v, tag);
    u32x2 us[4];
    const u32 yo = (ch * 2048 + 16 * l15 + 4 * h) * 2;
    const u32 t0 = hm ? 512u : 0u, t1 = hm ? 1024u : 1536u, t2 = hm ? 2560u : 2048u, t3 = hm ? 3072u : 3584u;
    u32x4 ua, ub; u32 uc;
    {
      const u32 uo = WS_XNT + (ch * 2048 + tc * 16) * 2;
      ua = __builtin_amdgcn_raw_buffer_load_b128(rs_ws, uo, 0, 16);
      ub = __builtin_amdgcn_raw_buffer_load_b128(rs_ws, uo + 16, 0, 16);
      uc = __builtin_amdgcn_raw_buffer_load_b32(rs_ws, uo + 32, 0, 16);
      us[0] = __builtin_amdgcn_raw_buffer_load_b64(rs_ws, yo, WS_XNT + t0, 16);
      us[1] = __builtin_amdgcn_raw_buffer_load_b64(rs_ws, yo, WS_XNT + t1, 16);
      us[2] = __builtin_amdgcn_raw_buffer_load_b64(rs_ws, yo, WS_XNT + t2, 16);
      us[3] = __builtin_amdgcn_raw_buffer_load_b64(rs_ws, yo, WS_XNT + t3, 16);
    }
    AWAIT(E_KLIN);
    {
      LAS char* kl = S + KLIN + cc * KLS;
      const float jf = (float)(2047 - 16 * tc);
      const bool low = tc >= 120;
      u32 ko[8];
#pragma unroll
      for (int q = 0; q < 8; ++q) {
        const u32 bits = pkbf(jf - (float)(2 * q), jf - (float)(2 * q + 1));
        const u32 j0 = 2047 - 16 * tc - 2 * q;
        const u32 pr = low ? (j0 | ((j0 - 1) << 16)) : bits - 0x42804280u;
        ko[q] = (u32)*(LAS u16*)(kl + (pr & 0xffff) * 2) | ((u32)*(LAS u16*)(kl + (pr >> 16) * 2) << 16);
      }
      LAS char* kr = opq(S + KRO + cc * KRS + tc * 32);
      *(LAS u32x4*)(kr) = u32x4{ko[0], ko[1], ko[2], ko[3]};
      *(LAS u32x4*)(kr + 16) = u32x4{ko[4], ko[5], ko[6], ko[7]};
      *(LAS u32x4*)(S + KRO + cc * KRS + 4096 + min(tc, 29) * 16) = u32x4{0, 0, 0, 0};
    }
    {
      u32 d[9], o[8];
#pragma unroll
      for (int q = 0; q < 4; ++q) { d[q] = pkh(bflo(ua[q]), bfhi(ua[q])); d[4 + q] = pkh(bflo(ub[q]), bfhi(ub[q])); }
      d[8] = pkh(bflo(uc), 0.f);
#pragma unroll
      for (int q = 0; q < 8; ++q) o[q] = __builtin_amdgcn_alignbit(d[q + 1], d[q], 16);
      LAS char* ucb = opq(S + UC + cc * 2 * UCS + (2 * tc + 2) * 16);
#pragma unroll
      for (int k = 0; k < 2; ++k) {
        *(LAS u32x4*)(ucb + k * 16) = u32x4{d[4 * k], d[4 * k + 1], d[4 * k + 2], d[4 * k + 3]};
        *(LAS u32x4*)(ucb + k * 16 + UCS) = u32x4{o[4 * k], o[4 * k + 1], o[4 * k + 2], o[4 * k + 3]};
      }
      if (tc == 0) {
        LAS char* z = ucb - 32;
        *(LAS u32x4*)(z) = u32x4{0, 0, 0, 0};
        *(LAS u32x4*)(z + UCS) = u32x4{0, 0, 0, 0};
        *(LAS u32x4*)(z + 16) = u32x4{0, 0, 0, 0};
        *(LAS u32x4*)(z + UCS + 16) = u32x4{0, 0, 0, d[0] << 16};
      }
    }
    ARRIVE(E_UC);
    AWAIT(E_UC);
    {
#pragma unroll
      for (int q = 0; q < 4; ++q) ca[q] = f32x4{0, 0, 0, 0};
      const int v = l15 + 1, sft = v & 1;
      LAS char* ub0 = S + UC + (cc * 2 + sft) * UCS + (8 * h + v - sft) * 2;
      LAS char* ub[4];
#pragma unroll
      for (int q = 0; q < 4; ++q) ub[q] = opq(ub0 + q * 1024 - 64);
      LAS char* kbase = opq(S + KRO + cc * KRS + (2032 - 1792 - 16 * l15 + 8 * h) * 2);
      auto cv = [&](auto Gc) {
        constexpr int G = CV(Gc);
        constexpr int TA = G ? 1 : 0, TB = G ? 2 : 3, TC = G ? 5 : 4, TD = G ? 6 : 7;
        u32x4 U[2][TD + 1], Kf[2][TD + 1];
        auto ldu = [&](int i, int r) {
          LAS u32* p = (LAS u32*)(ub[i >> 1] + (i & 1) * 512 + r * 64);
          return u32x4{p[0], p[1], p[2], p[3]};
        };
#pragma unroll
        for (int i = 0; i <= TD; ++i) { U[0][i] = ldu(i, 1); Kf[0][TD - i] = *(LAS u32x4*)(kbase + (7 - (TD - i)) * 512); }
#pragma unroll 1
        for (int rp = 0; rp < 4; ++rp) {
          static_for<0, 2>([&](auto pc) {
            constexpr int p = CV(pc);
            static_for<0, TD + 1>([&](auto ic) {
              constexpr int i = CV(ic);
              SB();
              ca[3] = mfma_h(U[p][i], Kf[p][TD - i], ca[3]);
              SB();
              U[p ^ 1][i] = ldu(i, p + 2);
              SB();
              if constexpr (i >= TD - TC) ca[2] = mfma_h(U[p][i - (TD - TC)], Kf[p][TD - i], ca[2]);
              SB();
              Kf[p ^ 1][TD - i] = *(LAS u32x4*)(kbase + (p + 1) * 64 + (7 - (TD - i)) * 512);
              SB();
              if constexpr (i >= TD - TB) ca[1] = mfma_h(U[p][i - (TD - TB)], Kf[p][TD - i], ca[1]);
              if constexpr (i >= TD - TA) ca[0] = mfma_h(U[p][i - (TD - TA)], Kf[p][TD - i], ca[0]);
            });
          });
          SB();
#pragma unroll
          for (int q = 0; q < 4; ++q) ub[q] = opq(ub[q] + 128);
          kbase = opq(kbase + 128);
        }
      };
      if (hm == 0) cv(std::integral_constant<int, 0>{}); else cv(std::integral_constant<int, 1>{});
    }
    const float dsk = cc ? bfhi(dsk2) : bflo(dsk2);
    u32 tsk[4][2];
#pragma unroll
    for (int q = 0; q < 4; ++q) {
      tsk[q][0] = pkbf(bflo(us[q][0]) * dsk, bfhi(us[q][0]) * dsk);
      tsk[q][1] = pkbf(bflo(us[q][1]) * dsk, bfhi(us[q][1]) * dsk);
    }
    ARRIVE(E_CONV);
#pragma unroll
    for (int q = 0; q < 4; ++q) {
      const f32x4 a = ca[q];
      u32 y01 = pkbf(a[0] * yscale, a[1] * yscale), y23 = pkbf(a[2] * yscale, a[3] * yscale);
      u32x2 o = {pkbf(bflo(y01) + bflo(tsk[q][0]), bfhi(y01) + bfhi(tsk[q][0])), pkbf(bflo(y23) + bflo(tsk[q][1]), bfhi(y23) + bfhi(tsk[q][1]))};
      __builtin_amdgcn_raw_buffer_store_b64(o, rs_ws, yo, WS_YT + (q == 0 ? t0 : q == 1 ? t1 : q == 2 ? t2 : t3), 16);
    }
    AWAIT(E_W2);
    w2_regs();
    WAITV(0);
    if (l == 0) __hip_atomic_store((GAS u64*)(ws + WS_FY + (b * 4 + q4) * FSTR), tag, RLX_AGENT);
    w2_req();
    if (b == 0 && w == 7) {
      GAS u64* p = (GAS u64*)(ws + WS_FXN + (size_t)l * 4 * FSTR);
      const u64 v0 = probe(p), v1 = probe(p + 64 * 4 * FSTR / 8), v2 = probe(p + 128 * 4 * FSTR / 8), v3 = probe(p + 192 * 4 * FSTR / 8);
      wait_flags(p, v0, tag); wait_flags(p + 64 * 4 * FSTR / 8, v1, tag);
      wait_flags(p + 128 * 4 * FSTR / 8, v2, tag); wait_flags(p + 192 * 4 * FSTR / 8, v3, tag);
      if (l == 0) __hip_atomic_store((GAS u64*)ws, tag, RLX_AGENT);
    }
    __builtin_amdgcn_s_setprio(0);
  }

  {
    GAS u64* fact_p = (GAS u64*)(ws + WS_FACT + (((bp >> 1) * 16 + 2 * w + (l & 1)) * 8 + 2 * (bp & 1) + ((l >> 1) & 1)) * FSTR);
    wait_flags(fact_p, probe(fact_p), tag);
    const u32 ao = WS_ACT + (4 * bp * 56 + 7 * w) * 1024 + l * 16;
    u32x4 af[7][4];
#define KORD(i) ((i) < 2 ? (i) : (i) < 4 ? (i) + 2 : (i) < 6 ? (i) - 2 : 6)
    auto lda = [&](auto ic, auto lc) {
      af[CV(ic)][CV(lc)] = __builtin_amdgcn_raw_buffer_load_b128(rs_ws, ao, (CV(lc) * 56 + KORD(CV(ic))) * 1024, 16);
    };
    static_for<0, 3>([&](auto ic) { static_for<0, 4>([&](auto lc) { lda(ic, lc); }); });
    u64 fact_v = probe(fact_p + 4 * FSTR / 8);
    f32x4 ac[4][4];
#pragma unroll
    for (int dt = 0; dt < 4; ++dt)
#pragma unroll
      for (int lt = 0; lt < 4; ++lt) ac[dt][lt] = f32x4{0, 0, 0, 0};
    const int yT = bp >> 2;
    GAS u64* fy_p = (GAS u64*)(ws + WS_FY + ((32 * bc + 8 * q4 + ((l >> 1) & 7)) * 4 + (l & 1) * 2 + ((yT ^ (yT >> 1)) & 1)) * FSTR);
    u64 fy_v = 0;
    u32x4 xres;
    static_for<0, 7>([&](auto ic) {
      constexpr int i = CV(ic);
      u32x4 wf[4];
#pragma unroll
      for (int dt = 0; dt < 4; ++dt) {
        if constexpr (i < 4) wf[dt] = wr1[i][dt];
        else wf[dt] = w2_frag(dt, i - 4);
      }
      if constexpr (i == 1) wait_flags(fact_p + 4 * FSTR / 8, fact_v, tag);
      static_for<0, 4>([&](auto lc) {
        constexpr int lt = CV(lc);
        SB();
#pragma unroll
        for (int dt = 0; dt < 4; ++dt) ac[dt][lt] = mfma_bf(wf[dt], af[i][lt], ac[dt][lt]);
        SB();
        if constexpr (i + 3 < 7) lda(std::integral_constant<int, i + 3>{}, lc);
        if constexpr (i == 4 && lt == 0) {
          xres = __builtin_amdgcn_raw_buffer_load_b128(rs_x, (64 * bp + (t >> 3)) * 1024 + bc * 128 + (t & 7) * 16, 0, 0);
          if (w >= 4) fy_v = probe(fy_p);
        }
      });
    });
    u32x4 y0, y1;
    if (w >= 4) {
      wait_flags(fy_p, fy_v, tag);
      const u32 yoff = WS_YT + ((64 * bc + 16 * q4 + (l >> 3)) * 2048 + 64 * bp + (l & 7) * 8) * 2;
      y0 = __builtin_amdgcn_raw_buffer_load_b128(rs_ws, yoff, 0, 16);
      y1 = __builtin_amdgcn_raw_buffer_load_b128(rs_ws, yoff, 8 * 4096, 16);
    }
    AWAIT(E_CONV);
#pragma unroll
    for (int dt = 0; dt < 4; ++dt) {
      const int sg = ((2 * dt + (h >> 1)) & 3) * 4;
      LAS char* pw = opq(S + dt * STAGE + 4096 * w + h * 256 + ((l15 ^ sg) * 16));
#pragma unroll
      for (int lt = 0; lt < 4; ++lt) *(LAS f32x4*)(pw + lt * 1024) = ac[dt][lt];
    }
    if (w >= 4) {
      LAS char* yw = opq(S + YI + (16 * q4 + (l >> 3)) * 128 + (((l & 7) ^ (2 * q4)) * 16));
      *(LAS u32x4*)(yw) = y0;
      *(LAS u32x4*)((LAS char*)((size_t)yw ^ 16) + 1024) = y1;
    }
    BAR();
    {
      const int lrow = t >> 3, g = t & 7;
      LAS char* pr = opq(S + (g >> 1) * STAGE + (lrow >> 4) * 1024 + (2 * (g & 1)) * 256 + (((lrow & 15) ^ ((g & 3) * 4)) * 16));
      LAS char* yr = opq(S + YI + 8 * g * 128 + (((lrow >> 3) ^ g) * 16) + (lrow & 7) * 2);
      f32x4 s0 = *(LAS f32x4*)(pr), s1 = *(LAS f32x4*)(pr + 256);
#pragma unroll
      for (int p = 1; p < 8; ++p) {
        s0 += *(LAS f32x4*)(pr + 4096 * p);
        s1 += *(LAS f32x4*)(pr + 4096 * p + 256);
      }
      float yb[8] = {s0[0], s0[1], s0[2], s0[3], s1[0], s1[1], s1[2], s1[3]};
      u32 o[4];
#pragma unroll
      for (int q = 0; q < 4; ++q) {
        u32 y0 = *(LAS u16*)(yr + (2 * q) * 128);
        u32 y1 = *(LAS u16*)(yr + (2 * q + 1) * 128);
        u32 a = pkbf(bflo(xres[q]) + bflo(y0), bfhi(xres[q]) + bflo(y1));
        u32 bq = pkbf(yb[2 * q], yb[2 * q + 1]);
        o[q] = pkbf(bflo(a) + bflo(bq), bfhi(a) + bfhi(bq));
      }
      __builtin_amdgcn_raw_buffer_store_b128(u32x4{o[0], o[1], o[2], o[3]}, rs_out, (64 * bp + lrow) * 1024 + bc * 128 + g * 16, 0, 0);
    }
  }
}

#if defined(__HIP_DEVICE_COMPILE__)
#pragma clang attribute pop
#endif

extern "C" void kernel_launch(void* const* d_in, const int* in_sizes, int n_in, void* d_out, int out_size, void* d_ws,
                              size_t ws_size, hipStream_t stream) {
  Params P;
  P.x = (const u16*)d_in[0];
  P.Are = (const u16*)d_in[1];
  P.Aim = (const u16*)d_in[2];
  P.Bre = (const u16*)d_in[3];
  P.Bim = (const u16*)d_in[4];
  P.Cre = (const u16*)d_in[5];
  P.Cim = (const u16*)d_in[6];
  P.logdt = (const u16*)d_in[7];
  P.dskip = (const u16*)d_in[8];
  P.w1 = (const u16*)d_in[10];
  P.b1 = (const u16*)d_in[11];
  P.w2 = (const u16*)d_in[12];
  P.out = (u16*)d_out;
  P.ws = (char*)d_ws;
  (void)hipFuncSetAttribute((const void*)fused_kernel, hipFuncAttributeMaxDynamicSharedMemorySize, LDS_BYTES);
  fused_kernel<<<256, 512, LDS_BYTES, stream>>>(P);
}
```

```cpp
#if defined(__HIP_DEVICE_COMPILE__)
#pragma clang attribute push(__attribute__((target("no-packed-fp32-ops"))), apply_to = function)
#endif
#include <hip/hip_runtime.h>
#include <stdint.h>
#include <type_traits>

typedef unsigned short u16;
typedef unsigned int u32;
typedef unsigned long long u64;
typedef short s16x4 __attribute__((ext_vector_type(4)));
typedef _Float16 h16x2 __attribute__((ext_vector_type(2)));
typedef _Float16 h16x8 __attribute__((ext_vector_type(8)));
typedef __bf16 b16x2 __attribute__((ext_vector_type(2)));
typedef __bf16 b16x8 __attribute__((ext_vector_type(8)));
typedef float f32x2 __attribute__((ext_vector_type(2)));
typedef float f32x4 __attribute__((ext_vector_type(4)));
typedef u32 u32x2 __attribute__((ext_vector_type(2)));
typedef u32 u32x4 __attribute__((ext_vector_type(4)));
typedef int i32x4 __attribute__((ext_vector_type(4)));
#define LAS __attribute__((address_space(3)))
#define GAS __attribute__((address_space(1)))
#define RLX_AGENT __ATOMIC_RELAXED, __HIP_MEMORY_SCOPE_AGENT

constexpr int L_ = 2048, D_ = 512, N_ = 128, F_ = 1792;
#define CONV_SPLIT 0
#define S_PRIO 0
#define POLL_SLEEP 8
#ifndef M_PRIO_E1
#define M_PRIO_E1 2
#endif
#define PRETOUCH 0
#define CHUNK 0
#define S2_BY_S 1
#define ROT 0
#define CODE_PF_BYTES 24576

constexpr u32 WS_STAT = 4096;
constexpr u32 FSTR = 128;
constexpr u32 WS_FXN = 32768;
constexpr u32 WS_FACT = WS_FXN + 1024 * FSTR;
constexpr u32 WS_FY = WS_FACT + 2048 * FSTR;
constexpr u32 WS_XNT = 1048576;
constexpr u32 WS_ACT = WS_XNT + 2097152;
constexpr u32 WS_YT = WS_ACT + 7340032;
constexpr u32 WS_END = WS_YT + 2097152;
#define DBG_OFF (16u << 20)

constexpr u32 STAGE = 32768;
constexpr u32 KT = 131072;
constexpr u32 KTW = 4352;
constexpr u32 XT = 148480;
constexpr u32 KX = 148480;
constexpr u32 KRO = 148480;
constexpr u32 KRS = 4576;
constexpr u32 DUM = 157632;
constexpr u32 SYN = 158656;
constexpr u32 CSO = 158784;
constexpr u32 STO = 129024;
constexpr u32 B1O = 129536;
constexpr u32 KLIN = 161248;
constexpr u32 KLS = 1296;
constexpr u32 UC = 131072;
constexpr u32 UCS = 4160;
constexpr u32 CRED = 131584;
constexpr u32 YI = 131584;
constexpr u32 LDS_BYTES = 163840;
enum { C_LANDED = 0, E_XT = 16, E_XTR, E_XNST, E_KX, E_KLIN, E_UC, E_CONV, E_YST, E_CS, E_ACT, E_CRED, E_W2, E_G2 };

constexpr int SPIN_CAP = 1 << 16;

__device__ __forceinline__ float bflo(u32 v) { return __uint_as_float(v << 16); }
__device__ __forceinline__ float bfhi(u32 v) { return __uint_as_float(v & 0xffff0000u); }
__device__ __forceinline__ u32 pkbf(float a, float b) {
  f32x2 v = {a, b};
  return __builtin_bit_cast(u32, __builtin_convertvector(v, b16x2));
}
__device__ __forceinline__ u32 pkh(float a, float b) {
  f32x2 v = {a, b};
  return __builtin_bit_cast(u32, __builtin_convertvector(v, h16x2));
}
__device__ __forceinline__ float rbf(float a) { return bflo(pkbf(a, 0.f)); }

__device__ __forceinline__ i32x4 mk_srd(const void* p, u32 bytes) {
  u64 a = (u64)p;
  i32x4 r;
  r[0] = (int)(u32)a;
  r[1] = (int)((u32)(a >> 32) & 0xffffu);
  r[2] = (int)bytes;
  r[3] = 0x00020000;
  return r;
}
__device__ __forceinline__ void dma16(i32x4 srd, u32 voff, u32 soff, u32 dst) {
  asm volatile("s_mov_b32 m0, %3\n\ts_nop 0\n\tbuffer_load_dwordx4 %0, %1, %2 offen lds"
               :: "v"(voff), "s"(srd), "s"(__builtin_amdgcn_readfirstlane(soff)), "s"(__builtin_amdgcn_readfirstlane(dst)));
}
__device__ __forceinline__ void dma4(i32x4 srd, u32 voff, u32 dst) {
  asm volatile("s_mov_b32 m0, %2\n\ts_nop 0\n\tbuffer_load_dword %0, %1, 0 offen lds"
               :: "v"(voff), "s"(srd), "s"(__builtin_amdgcn_readfirstlane(dst)));
}
__device__ __forceinline__ u32 ald4(i32x4 srd, u32 voff) {
  u32 r;
  asm volatile("buffer_load_dword %0, %1, %2, 0 offen" : "=v"(r) : "v"(voff), "s"(srd) : "memory");
  return r;
}
__device__ __forceinline__ u32x2 ald8(i32x4 srd, u32 voff) {
  u32x2 r;
  asm volatile("buffer_load_dwordx2 %0, %1, %2, 0 offen" : "=v"(r) : "v"(voff), "s"(srd) : "memory");
  return r;
}
__device__ __forceinline__ u32x4 ald16(i32x4 srd, u32 voff) {
  u32x4 r;
  asm volatile("buffer_load_dwordx4 %0, %1, %2, 0 offen" : "=v"(r) : "v"(voff), "s"(srd) : "memory");
  return r;
}
#define BAR() asm volatile("s_waitcnt lgkmcnt(0)\n\ts_barrier" ::: "memory")
#define WAITV(n) asm volatile("s_waitcnt vmcnt(" #n ")" ::: "memory")
#define SB() __builtin_amdgcn_sched_barrier(0)

__device__ __forceinline__ f32x4 mfma_bf(u32x4 a, u32x4 b, f32x4 c) {
  return __builtin_amdgcn_mfma_f32_16x16x32_bf16(__builtin_bit_cast(b16x8, a), __builtin_bit_cast(b16x8, b), c, 0, 0, 0);
}
__device__ __forceinline__ f32x4 mfma_h(u32x4 a, u32x4 b, f32x4 c) {
  return __builtin_amdgcn_mfma_f32_16x16x32_f16(__builtin_bit_cast(h16x8, a), __builtin_bit_cast(h16x8, b), c, 0, 0, 0);
}
__device__ __forceinline__ u32x2 tr_rd(LAS char* p) {
  return __builtin_bit_cast(u32x2, __builtin_amdgcn_ds_read_tr16_b64_v4i16((LAS s16x4*)p));
}
__device__ __forceinline__ u32x4 cat(u32x2 a, u32x2 b) { return u32x4{a[0], a[1], b[0], b[1]}; }
template <int CTRL> __device__ __forceinline__ float dpp_mov(float v) {
  return __builtin_bit_cast(float, __builtin_amdgcn_update_dpp(0, __builtin_bit_cast(int, v), CTRL, 0xf, 0xf, true));
}
__device__ __forceinline__ float rdlane(float v, int i) {
  return __builtin_bit_cast(float, __builtin_amdgcn_readlane(__builtin_bit_cast(int, v), i));
}
__device__ __forceinline__ float wave_max(float v) {
  v = fmaxf(v, dpp_mov<0xB1>(v));
  v = fmaxf(v, dpp_mov<0x4E>(v));
  v = fmaxf(v, dpp_mov<0x141>(v));
  v = fmaxf(v, dpp_mov<0x140>(v));
  return fmaxf(fmaxf(rdlane(v, 0), rdlane(v, 16)), fmaxf(rdlane(v, 32), rdlane(v, 48)));
}
__device__ __forceinline__ float wave_sum(float v) {
  v += dpp_mov<0xB1>(v);
  v += dpp_mov<0x4E>(v);
  v += dpp_mov<0x141>(v);
  v += dpp_mov<0x140>(v);
  return (rdlane(v, 0) + rdlane(v, 16)) + (rdlane(v, 32) + rdlane(v, 48));
}
__device__ __forceinline__ u64 probe(GAS u64* p) { return __hip_atomic_load(p, RLX_AGENT); }
__device__ __forceinline__ void wait_flags(GAS u64* p, u64 v, u64 tag) {
  if (!__all(v == tag)) {
#pragma unroll 1
    for (int it = 0; it < SPIN_CAP; ++it) {
      __builtin_amdgcn_s_sleep(POLL_SLEEP);
      v = __hip_atomic_load(p, RLX_AGENT);
      if (__all(v == tag)) break;
    }
  }
  __builtin_amdgcn_fence(__ATOMIC_ACQUIRE, "wavefront");
}
template <int I, int N, class F> __device__ __forceinline__ void static_for(F&& f) {
  if constexpr (I < N) { f(std::integral_constant<int, I>{}); static_for<I + 1, N>(f); }
}
#define CV(x) decltype(x)::value
__device__ __forceinline__ void lds_arrive(u32 addr) {
  u32 one = 1;
  asm volatile("s_mov_b64 exec, 1\n\tds_add_u32 %0, %1\n\ts_mov_b64 exec, -1" :: "v"(addr), "v"(one) : "memory");
}
__device__ __forceinline__ void lds_wait(LAS char* p, u32 target) {
#pragma unroll 1
  for (int it = 0; it < (1 << 22); ++it) {
    u32 v = *(volatile LAS u32*)p;
    if ((u32)__builtin_amdgcn_readfirstlane(v) >= target) break;
    __builtin_amdgcn_s_sleep(1);
  }
  asm volatile("" ::: "memory");
}
__device__ __forceinline__ void lds_spin(LAS char* p, u32 target) {
#pragma unroll 1
  for (int it = 0; it < (1 << 24); ++it) {
    u32 v = *(volatile LAS u32*)p;
    if ((u32)__builtin_amdgcn_readfirstlane(v) >= target) break;
  }
  asm volatile("" ::: "memory");
}
__device__ __forceinline__ LAS char* opq(LAS char* p) { asm volatile("" : "+v"(p)); return p; }
__device__ __forceinline__ float half_sum(float v) {
  v += dpp_mov<0xB1>(v);
  v += dpp_mov<0x4E>(v);
  v += dpp_mov<0x141>(v);
  v += dpp_mov<0x140>(v);
  auto r = __builtin_amdgcn_permlane16_swap(__builtin_bit_cast(u32, v), __builtin_bit_cast(u32, v), false, false);
  return __builtin_bit_cast(float, (u32)r[0]) + __builtin_bit_cast(float, (u32)r[1]);
}
struct cpx { float re, im; };
__device__ __forceinline__ cpx cmul(cpx a, cpx b) {
  cpx r;
  r.re = fmaf(a.re, b.re, -(a.im * b.im));
  r.im = fmaf(a.re, b.im, a.im * b.re);
  return r;
}

#define STAMP(k)

struct Params {
  const u16 *x, *Are, *Aim, *Bre, *Bim, *Cre, *Cim, *logdt, *dskip, *w1, *b1, *w2;
  u16* out;
  char* ws;
};

__global__ void __launch_bounds__(512, 2) fused_kernel(Params P) {
  extern __shared__ __attribute__((aligned(16))) char smem[];
  LAS char* S = (LAS char*)smem;
  const u32 sbase = (u32)(size_t)S;
  char* ws = P.ws;
  const int t = __builtin_amdgcn_workitem_id_x(), l = t & 63, w = __builtin_amdgcn_readfirstlane(t >> 6);
  const int h = l >> 4, l15 = l & 15;
  const int b = __builtin_amdgcn_workgroup_id_x(), xcd = b & 7, rk = b >> 3;
  const int bi = 4 * (xcd >> 1) + (rk >> 3), bj = 8 * (xcd & 1) + (rk & 7);
  const int bp = 4 * xcd + (rk >> 3), bc = rk & 7;
  const int q4 = w & 3;
  STAMP(0);
  if (w == 7) dma16(mk_srd((const void*)__builtin_amdgcn_s_getpc(), CODE_PF_BYTES), rk * 1024 + l * 16, 0, sbase + DUM);

  const __amdgpu_buffer_rsrc_t rs_ws = __builtin_amdgcn_make_buffer_rsrc((void*)ws, 0, WS_END, 0x00020000);
  const __amdgpu_buffer_rsrc_t rs_x = __builtin_amdgcn_make_buffer_rsrc((void*)P.x, 0, L_ * D_ * 2, 0x00020000);
  const __amdgpu_buffer_rsrc_t rs_out = __builtin_amdgcn_make_buffer_rsrc((void*)P.out, 0, L_ * D_ * 2, 0x00020000);
  const i32x4 sx = mk_srd(P.x, L_ * D_ * 2), sw1 = mk_srd(P.w1, D_ * F_ * 2), sw2 = mk_srd(P.w2, D_ * F_ * 2);
  u64 tag;
  u32 ldt2, dsk2;
  asm volatile("s_load_dwordx2 %0, %3, 0x0\n\ts_load_dword %1, %4, 0x0\n\ts_load_dword %2, %5, 0x0"
               : "=&s"(tag), "=&s"(ldt2), "=&s"(dsk2) : "s"(ws), "s"(P.logdt + 2 * b), "s"(P.dskip + 2 * b));

  u32 p_ar, p_ai, p_br, p_bi, p_cr, p_ci;
  u32x4 sr0, sr1;
  const int cc = q4 >> 1, hm = q4 & 1, ch = 2 * b + cc;
  if (w < 4) {
    if (t < 32) *(LAS u32*)(S + SYN + t * 4) = 0;
  } else {
    const u32 o = (128 * bi + 8 * bj + 2 * q4) * 1024 + l * 32;
    sr0 = ald16(sx, o);
    sr1 = ald16(sx, o + 16);
    const u32 po = ch * 256 + 4 * l;
    p_ar = ald4(mk_srd(P.Are, D_ * N_ * 2), po);
    p_ai = ald4(mk_srd(P.Aim, D_ * N_ * 2), po);
    p_br = ald4(mk_srd(P.Bre, D_ * N_ * 2), po);
    p_bi = ald4(mk_srd(P.Bim, D_ * N_ * 2), po);
    p_cr = ald4(mk_srd(P.Cre, D_ * N_ * 2), po);
    p_ci = ald4(mk_srd(P.Cim, D_ * N_ * 2), po);
  }
  const u32 dq = q4 < 3 ? 4096u : 12288u, dq2 = q4 < 3 ? 11264u : 12288u;
  const u32 vx = (128 * bi + 8 * q4 + (l >> 3)) * 1024 + (((l & 7) ^ (l >> 3)) * 16);
  u32 vwa, vwb;
  {
    const u32 g0 = q4 * 64 + l, pos0 = (g0 * 4682u) >> 16, c0 = g0 - pos0 * 14;
    const u32 g1 = g0 + 256, pos1 = (g1 * 4682u) >> 16, c1 = g1 - pos1 * 14;
    const u32 k0 = (pos0 & ~12u) | ((pos0 & 4) << 1) | ((pos0 & 8) >> 1);
    const u32 k1 = (pos1 & ~12u) | ((pos1 & 4) << 1) | ((pos1 & 8) >> 1);
    vwa = k0 * (F_ * 2) + bj * 224 + c0 * 16;
    vwb = q4 < 3 ? k1 * (F_ * 2) + bj * 224 + c1 * 16 : 0x7ff00000u;
  }
  auto issue_stage = [&](int c) {
    const u32 sb_ = sbase + c * STAGE + q4 * 1024;
#pragma unroll
    for (int i = 0; i < 4; ++i) dma16(sx, vx, c * 128 + i * 32768, sb_ + i * 4096);
    const u32 so = c * (64 * F_ * 2);
    dma16(sw1, vwa, so, sb_ + 16384);
    dma16(sw1, vwa, so + 32 * F_ * 2, sb_ + 16384 + 7168);
    dma16(sw1, vwb, so, sb_ + 16384 + dq);
    dma16(sw1, vwb, so + 32 * F_ * 2, sb_ + 16384 + dq2);
  };
  if (w < 4) issue_stage(0);
  BAR();
  asm volatile("" : "+s"(tag), "+s"(ldt2), "+s"(dsk2));
  tag += 1ull;
  issue_stage(1 + (w >> 2));
  u32 w2o;
  LAS char* w2a[4];
  {
    const int rr = l >> 3, slot = l & 7;
    const int dt = (slot >> 1) ^ ((rr >> 1) & 3);
    w2o = (224 * w + rr) * 1024 + bc * 128 + (2 * dt + (slot & 1)) * 16;
    const int q = l15 >> 2, p = l & 3, x = (2 * h + (q >> 1)) & 3;
#pragma unroll
    for (int d = 0; d < 4; ++d) w2a[d] = S + 4096 * w + (4 * h + q) * 128 + ((d ^ x) * 32) + (p >> 1) * 16 + (p & 1) * 8;
  }
#define W2TILE(ks, hf) ((ks) < 3 ? 2 * (ks) + (hf) : ((ks) == 3 ? ((hf) ? 13 : 6) : 7 + 2 * ((ks) - 4) + (hf)))
#define ISSUE_W2(p, n, slot) dma16(sw2, w2o, (W2TILE(p, (n) >> 1) * 16 + ((n) & 1) * 8) * 1024, sbase + (slot) * STAGE + 4096 * w + (n) * 1024)
#define ISSUE_W2P(p, n, slot) dma16(sw2, w2o, (W2TILE(p, (n) >> 1) * 16 + ((n) & 1) * 8 + 896) * 1024, sbase + (slot) * STAGE + 4096 * w + (n) * 1024)
  auto w2_frag = [&](int d, int slot, int extra = 0) {
    LAS char* p = (slot >= 2 ? opq(w2a[d] + 2 * STAGE) + (slot - 2) * STAGE : w2a[d] + slot * STAGE) + extra;
    return cat(tr_rd(p), tr_rd(p + 2048));
  };
  u32x4 wr1[4][4];
  auto w2_regs = [&]() {
#pragma unroll
    for (int k = 0; k < 4; ++k)
#pragma unroll
      for (int d = 0; d < 4; ++d) wr1[k][d] = w2_frag(d, k);
    asm volatile("s_waitcnt lgkmcnt(0)" ::: "memory");
  };
  auto w2_req = [&]() {
#pragma unroll
    for (int n = 0; n < 4; ++n) ISSUE_W2(2, n, 0);
#pragma unroll
    for (int n = 0; n < 4; ++n) ISSUE_W2(3, n, 1);
#pragma unroll
    for (int n = 0; n < 4; ++n) ISSUE_W2(6, n, 2);
#pragma unroll
    for (int n = 0; n < 4; ++n) ISSUE_W2P(2, n, 3);
  };
  f32x4 ca[4];
  const u32 syn = sbase + SYN;
  LAS char* synp = opq(S + SYN);
#define ARRIVE(e) lds_arrive(syn + (e) * 4)
#define AWAIT(e) lds_wait(synp + (e) * 4, 4)

  if (w < 4) {
    f32x4 acc[7][2], cs0 = {0, 0, 0, 0}, cs1 = {0, 0, 0, 0};
#pragma unroll
    for (int f = 0; f < 7; ++f) { acc[f][0] = f32x4{0, 0, 0, 0}; acc[f][1] = f32x4{0, 0, 0, 0}; }
    u32 xa = (32 * q4 + l15) * 128 + ((h ^ (l & 7)) * 16);
    u32 wo = 16384 + (16 * (h >> 1) + 4 * (h & 1) + (l15 >> 2)) * 224 + (l & 3) * 8;
    u32x4 ones = {0x3f803f80u, 0x3f803f80u, 0x3f803f80u, 0x3f803f80u};
    asm volatile("" : "+v"(ones));
    u32x4 xf[2][2];
    u32x2 wl[2][7], wh[2][7];
    u32 lnd = 0;
    auto kstep = [&](auto kc, auto mc, u32 so, u32 dst, u32 cnt_addr, LAS char* cnt_p, u32 tgt, int nwait) {
      constexpr int k = CV(kc), n = k ^ 1, MODE = CV(mc);
      constexpr int r0 = k ? 4 : 0;
      LAS char* xp = S + (k ? xa : (xa ^ 64));
      LAS char* wp = S + wo + (k ? 0 : 32 * 224);
      static_for<0, 14>([&](auto jc) {
        constexpr int j = CV(jc), f = j >> 1, mt = j & 1;
        SB();
        acc[f][mt] = mfma_bf(cat(wl[k][f], wh[k][f]), xf[k][mt], acc[f][mt]);
        SB();
        if constexpr (k == 1 && j == 1) lnd = *(volatile LAS u32*)cnt_p;
        if constexpr (k == 1 && j == 3) { if ((u32)__builtin_amdgcn_readfirstlane(lnd) < tgt) lds_spin(cnt_p, tgt); }
        if constexpr (j == r0) { xf[n][0] = *(LAS u32x4*)(xp); xf[n][1] = *(LAS u32x4*)(xp + 2048); }
        if constexpr (j >= r0 + 1 && j <= r0 + 7) {
          wl[n][j - r0 - 1] = tr_rd(wp + (j - r0 - 1) * 32);
          wh[n][j - r0 - 1] = tr_rd(wp + 8 * 224 + (j - r0 - 1) * 32);
        }
        constexpr int pc = j == (k ? 5 : 1) ? 0 : j == (k ? 8 : 5) ? 1 : j == (k ? 11 : 10) ? 2 : j == (k ? 13 : 12) ? 3 : -1;
        if constexpr (pc >= 0 && MODE == 1 && k == 0)
          dma16(sw1, pc < 2 ? vwa : vwb, so + (pc & 1) * (32 * F_ * 2), dst + (pc == 0 ? 0u : pc == 1 ? 7168u : pc == 2 ? dq : dq2));
        if constexpr (pc >= 0 && MODE == 1 && k == 1) dma16(sx, vx, so + pc * 32768, dst + pc * 4096);
        if constexpr (pc >= 0 && MODE == 2) dma16(sw2, w2o, so + pc * 8192, dst + pc * 1024);
        if constexpr (k == 0 && j == 8) {
          if (nwait == 2) WAITV(2); else if (nwait) WAITV(10);
          lds_arrive(cnt_addr);
        }
      });
      SB();
    };
    auto colsum = [&](auto kc) {
      constexpr int k = CV(kc);
      if (q4 < 2) {
        if (q4 == 0) { cs0 = mfma_bf(cat(wl[k][0], wh[k][0]), ones, cs0); cs1 = mfma_bf(cat(wl[k][4], wh[k][4]), ones, cs1); }
        else { cs0 = mfma_bf(cat(wl[k][1], wh[k][1]), ones, cs0); cs1 = mfma_bf(cat(wl[k][5], wh[k][5]), ones, cs1); }
      } else {
        if (q4 == 2) { cs0 = mfma_bf(cat(wl[k][2], wh[k][2]), ones, cs0); cs1 = mfma_bf(cat(wl[k][6], wh[k][6]), ones, cs1); }
        else cs0 = mfma_bf(cat(wl[k][3], wh[k][3]), ones, cs0);
      }
    };
    using I0 = std::integral_constant<int, 0>; using I1 = std::integral_constant<int, 1>; using I2 = std::integral_constant<int, 2>;
    WAITV(8);
    ARRIVE(C_LANDED);
    lds_spin(synp + C_LANDED * 4, 4);
    xf[0][0] = *(LAS u32x4*)(S + xa); xf[0][1] = *(LAS u32x4*)(S + xa + 2048);
#pragma unroll
    for (int f = 0; f < 7; ++f) { wl[0][f] = tr_rd(S + wo + f * 32); wh[0][f] = tr_rd(S + wo + 8 * 224 + f * 32); }
    auto adv = [&]() { xa = (xa + STAGE) & 131071u; wo = (wo + STAGE) & 131071u; };
#pragma unroll 1
    for (int c = 0; c < 5; ++c) {
      const u32 sl = sbase + ((c + 3) & 3) * STAGE + q4 * 1024;
      kstep(I0{}, I1{}, (c + 3) * (64 * F_ * 2), sl + 16384, syn + (c + 1) * 4, synp, 0, (S2_BY_S && c == 0) ? 2 : 10);
      colsum(I0{});
      adv();
      kstep(I1{}, I1{}, (c + 3) * 128, sl, 0, synp + (c + 1) * 4, (S2_BY_S && c == 1) ? 8u : 4u, 0);
      colsum(I1{});
    }
#pragma unroll 1
    for (int c = 5; c < 8; ++c) {
      const u32 sl = sbase + (c - 5) * STAGE + 4096 * w, wso = c < 7 ? (c - 5) * 32768 : 7 * 16384;
      kstep(I0{}, I2{}, wso, sl, syn + (c + 1) * 4, synp, 0, c < 7);
      colsum(I0{});
      adv();
      kstep(I1{}, I2{}, wso + 896 * 1024, sl + 16384, 0, synp + (c + 1) * 4, c < 7 ? 4u : 0u, 0);
      colsum(I1{});
    }
    if (l15 == 0) {
      LAS char* cp = opq(S + CSO + q4 * 64 + h * 16);
      *(LAS f32x4*)(cp) = cs0;
      *(LAS f32x4*)(cp + 256) = cs1;
    }
    AWAIT(E_XT);
    u32 pay2[2];
    u32x2 b1v[7];
    {
      LAS char* sp = opq(S + STO + (32 * q4 + l15) * 4);
      LAS char* bp_ = opq(S + B1O + h * 8);
      pay2[0] = *(LAS u32*)sp; pay2[1] = *(LAS u32*)(sp + 64);
#pragma unroll
      for (int f = 0; f < 7; ++f) b1v[f] = *(LAS u32x2*)(bp_ + 32 * f);
    }
    ARRIVE(E_CS);
    const float LOG2E = 1.4426950408889634f, LN2 = 0.6931471805599453f;
    float rs[2], tm[2];
#pragma unroll
    for (int mt = 0; mt < 2; ++mt) {
      rs[mt] = __builtin_amdgcn_rcpf(bfhi(pay2[mt])) * LOG2E;
      tm[mt] = -bflo(pay2[mt]) * rs[mt];
    }
    lds_spin(synp + E_CS * 4, 4);
    __builtin_amdgcn_s_setprio(M_PRIO_E1);
    const int jp = bj & 1;
    const u32 abase = WS_ACT + ((8 * bi + 2 * q4) * 56 + 7 * (bj >> 1) + (jp ? 4 : 0)) * 1024 + l * 16;
    LAS char* csp = opq(S + CSO + h * 16);
    u32x2 pk[2][2];
    static_for<0, 7>([&](auto fc) {
      constexpr int f = CV(fc);
      const f32x4 cs = *(LAS f32x4*)(csp + 64 * f);
      const float bia[4] = {bflo(b1v[f][0]) * LOG2E, bfhi(b1v[f][0]) * LOG2E, bflo(b1v[f][1]) * LOG2E, bfhi(b1v[f][1]) * LOG2E};
#pragma unroll
      for (int mt = 0; mt < 2; ++mt) {
        float a[4];
#pragma unroll
        for (int r = 0; r < 4; ++r) {
          float g = fmaf(acc[f][mt][r], rs[mt], fmaf(tm[mt], cs[r], bia[r]));
          float s1 = __builtin_amdgcn_exp2f(g) + 1.f;
          float rr = __builtin_amdgcn_rcpf(fmaf(s1, s1, 1.f));
          a[r] = g * fmaf(rr, -2.f * LN2, LN2);
        }
        pk[f & 1][mt] = u32x2{pkbf(a[0], a[1]), pkbf(a[2], a[3])};
        if constexpr (f < 2) {
          SB();
          const int n = 2 * f + mt;
          const u32 so = 9 * 16384 + n * 8192, dd = sbase + 3 * STAGE + 4096 * w + n * 1024;
          dma16(sw2, w2o, so, dd); dma16(sw2, w2o, so + 896 * 1024, dd + 16384);
          SB();
        }
      }
      if constexpr (f & 1) {
#pragma unroll
        for (int mt = 0; mt < 2; ++mt)
          __builtin_amdgcn_raw_buffer_store_b128(cat(pk[0][mt], pk[1][mt]), rs_ws, abase + (f >> 1) * 1024, mt * 56 * 1024, 16);
      }
      if constexpr (f == 6) {
#pragma unroll
        for (int mt = 0; mt < 2; ++mt)
          __builtin_amdgcn_raw_buffer_store_b64(pk[0][mt], rs_ws, abase + (jp ? -1024 + 8 : 3072), mt * 56 * 1024, 16);
      }
    });
    WAITV(6);
    ARRIVE(E_W2);
    w2_regs();
    WAITV(0);
    if (l == 0) __hip_atomic_store((GAS u64*)(ws + WS_FACT + ((bi * 16 + bj) * 8 + q4) * FSTR), tag, RLX_AGENT);
    __builtin_amdgcn_s_setprio(0);
    w2_req();
  } else {
    const int ts = t - 256;
    __builtin_amdgcn_s_setprio(S_PRIO);
    WAITV(8);
    asm volatile("" : "+v"(p_ar), "+v"(p_ai), "+v"(p_br), "+v"(p_bi), "+v"(p_cr), "+v"(p_ci));
    asm volatile("" : "+v"(sr0), "+v"(sr1));
    {
      float e[16];
#pragma unroll
      for (int q = 0; q < 4; ++q) { e[2 * q] = bflo(sr0[q]); e[2 * q + 1] = bfhi(sr0[q]); e[8 + 2 * q] = bflo(sr1[q]); e[9 + 2 * q] = bfhi(sr1[q]); }
      float s0 = (e[0] + e[1]) + (e[2] + e[3]), s1 = (e[4] + e[5]) + (e[6] + e[7]), s2 = (e[8] + e[9]) + (e[10] + e[11]), s3 = (e[12] + e[13]) + (e[14] + e[15]);
      const float mean = half_sum((s0 + s1) + (s2 + s3)) * (1.0f / D_);
      float v0 = 0, v1 = 0;
#pragma unroll
      for (int q = 0; q < 8; ++q) { float d0 = e[q] - mean, d1 = e[8 + q] - mean; v0 = fmaf(d0, d0, v0); v1 = fmaf(d1, d1, v1); }
      const float var = half_sum(v0 + v1) * (1.0f / D_);
      const float sd = __builtin_amdgcn_sqrtf(rbf(rbf(var) + 1e-5f));
      const u32 pay = pkbf(mean, sd);
      if ((l & 31) == 0) __hip_atomic_store((GAS u64*)(ws + WS_STAT + (128 * bi + 8 * bj + 2 * q4 + (l >> 5)) * 8), ((u64)(u32)tag << 32) | pay, RLX_AGENT);
    }
    WAITV(1);
    ARRIVE(C_LANDED + 2);
    u32x4 xs0 = ald16(sx, (128 * bi + (ts >> 1)) * 1024 + bj * 64 + (ts & 1) * 32);
    u32x4 xs1 = ald16(sx, (128 * bi + (ts >> 1)) * 1024 + bj * 64 + (ts & 1) * 32 + 16);
    u32 b1w = ald4(mk_srd(P.b1, F_ * 2), bj * 224 + 4 * l);
    float yscale, gmax, zr, zi;
    cpx cbn;
    {
      const float LOG2E = 1.4426950408889634f, INV2PI = 0.15915494309189535f;
      const float lx = cc ? bfhi(ldt2) : bflo(ldt2);
      const float th = lx * LOG2E, tl = fmaf(lx, LOG2E, -th) + lx * 1.925963033500011e-8f;
      const float dt = rbf(__builtin_amdgcn_exp2f(th) * fmaf(tl, 0.6931471805599453f, 1.0f));
      cpx cb0 = cmul(cpx{bflo(p_cr), bflo(p_ci)}, cpx{dt * bflo(p_br), dt * bflo(p_bi)});
      cpx cb1 = cmul(cpx{bfhi(p_cr), bfhi(p_ci)}, cpx{dt * bfhi(p_br), dt * bfhi(p_bi)});
      const float z0 = dt * bflo(p_ar) * LOG2E, z1 = dt * bfhi(p_ar) * LOG2E;
      gmax = fmaxf(wave_max(fmaxf(z0, z1)), 0.f);
      const float cbmax = wave_max(fmaxf(fmaxf(fabsf(cb0.re), fabsf(cb0.im)), fmaxf(fabsf(cb1.re), fabsf(cb1.im))));
      const float cbs = __uint_as_float((__float_as_uint(cbmax) + 0x007fffffu) & 0x7f800000u);
      const float icb = cbs > 0.f ? __builtin_amdgcn_rcpf(cbs) : 0.f;
      yscale = cbs * __builtin_amdgcn_exp2f(gmax * 2048.f) * (1.0f / 64.f);
      zr = fmaxf((hm ? z1 : z0) - gmax, -1e30f);
      zi = dt * (hm ? bfhi(p_ai) : bflo(p_ai)) * INV2PI;
      cbn = hm ? cb1 : cb0;
      cbn.re *= 64.f * icb; cbn.im *= -64.f * icb;
    }
    auto cexp = [&](float a) {
      float mg = __builtin_amdgcn_exp2f(zr * a);
      float ph = __builtin_amdgcn_fractf(zi * a);
      return cpx{mg * __builtin_amdgcn_cosf(ph), mg * __builtin_amdgcn_sinf(ph)};
    };
    const cpx e1 = cexp(1.f), e8 = cexp(8.f), e128 = cexp(128.f);
    GAS u64* stat_p = (GAS u64*)(ws + WS_STAT + (128 * bi + (ts >> 1)) * 8);
    u64 stat_v = 0;
    LAS char* tb = opq(S + KT + q4 * KTW + l * 4);
    LAS char* tf = opq(S + KT + q4 * KTW + l15 * 272 + h * 16);
    auto put = [&](int row, cpx v) { *(volatile LAS u32*)(tb + row * 272) = pkh(v.re, v.im); };
    u32x4 fa[4];
    f32x4 kacc[3];
    auto tile_mma = [&]() {
      f32x4 a = {0, 0, 0, 0};
#pragma unroll
      for (int k = 0; k < 4; ++k) a = mfma_h(fa[k], *(volatile LAS u32x4*)(tf + k * 64), a);
      return a;
    };
    {
      cpx wv = cbn;
      const cpx e8c = cpx{e8.re, -e8.im};
#pragma unroll
      for (int q = 0; q < 16; ++q) { put(q, wv); if (q < 15) wv = cmul(wv, e8c); if (q == 6) stat_v = probe(stat_p); }
#pragma unroll
      for (int k = 0; k < 4; ++k) fa[k] = *(volatile LAS u32x4*)(tf + k * 64);
    }
    if (!__all((u32)(stat_v >> 32) == (u32)tag)) {
#pragma unroll 1
      for (int it = 0; it < SPIN_CAP; ++it) {
        __builtin_amdgcn_s_sleep(16);
        stat_v = probe(stat_p);
        if (__all((u32)(stat_v >> 32) == (u32)tag)) break;
      }
    }
    WAITV(0);
    asm volatile("" : "+v"(xs0), "+v"(xs1), "+v"(b1w));
    {
      const u32 pay = (u32)stat_v;
      const float mb = bflo(pay), rsd = __builtin_amdgcn_rcpf(bfhi(pay));
      *(LAS u32*)(S + STO + (ts >> 1) * 4) = pay;
      *(LAS u32*)(S + B1O + l * 4) = b1w;
      LAS char* xt = opq(S + XT + (ts & 1) * 16 * 272 + (ts >> 1) * 2);
#pragma unroll
      for (int q = 0; q < 8; ++q) {
        const u32 xv = q < 4 ? xs0[q] : xs1[q - 4];
        u32 dd = pkbf(bflo(xv) - mb, bfhi(xv) - mb);
        u32 xn = pkbf(bflo(dd) * rsd, bfhi(dd) * rsd);
        *(LAS u16*)(xt + (2 * q) * 272) = (u16)xn;
        *(LAS u16*)(xt + (2 * q + 1) * 272) = (u16)(xn >> 16);
      }
      ARRIVE(E_XT);
    }
    cpx e2, e4, e6;
    {
      cpx lo = cpx{1.f, 0.f}, g = e128;
#pragma unroll
      for (int q = 0; q < 8; ++q) {
        put(q, lo); put(8 + q, g);
        if (q == 2) e2 = lo;
        if (q == 4) e4 = lo;
        if (q == 6) e6 = lo;
        if (q < 7) { lo = (q == 0) ? e1 : cmul(lo, e1); g = cmul(g, e1); }
        if (q == 1) {
          AWAIT(E_XT);
          LAS char* xr = opq(S + XT + (ts >> 4) * 272 + (ts & 15) * 16);
          u32x4 v0 = *(LAS u32x4*)(xr), v1 = *(LAS u32x4*)(xr + 16 * 272);
          const u32 xo = WS_XNT + ((32 * bj + (ts >> 4)) * 2048 + 128 * bi + (ts & 15) * 8) * 2;
          __builtin_amdgcn_raw_buffer_store_b128(v0, rs_ws, xo, 0, 16);
          __builtin_amdgcn_raw_buffer_store_b128(v1, rs_ws, xo, 16 * 4096, 16);
          ARRIVE(E_XTR);
        }
      }
      kacc[0] = tile_mma();
    }
    cpx hv = cmul(e128, e128);
#pragma unroll
    for (int a = 2; a < 8; ++a) {
      if (a < 4) {
        put((a - 2) * 4 + 0, hv); put((a - 2) * 4 + 1, cmul(hv, e2));
        put((a - 2) * 4 + 2, cmul(hv, e4)); put((a - 2) * 4 + 3, cmul(hv, e6));
      } else {
        put(8 + (a - 4) * 2 + 0, hv); put(8 + (a - 4) * 2 + 1, cmul(hv, e4));
      }
      hv = cmul(hv, e128);
      if (a == 3) {
        WAITV(0);
        if (l == 0) __hip_atomic_store((GAS u64*)(ws + WS_FXN + ((bj * 16 + bi) * 4 + q4) * FSTR), tag, RLX_AGENT);
      }
    }
    kacc[1] = tile_mma();
#pragma unroll
    for (int a = 8; a <= 16; ++a) { put(a - 8, hv); if (a < 16) hv = cmul(hv, e128); }
    GAS u64* fxn_p = (GAS u64*)(ws + WS_FXN + (((ch >> 5) * 16 + l15) * 4 + ((ch & 15) >> 2)) * FSTR);
    u64 fxn_v;
    kacc[2] = tile_mma();
    AWAIT(E_XTR);
    {
      LAS char* kx = opq(S + KX + q4 * 2048 + l * 16);
      *(LAS f32x4*)(kx) = hm ? kacc[0] : kacc[1];
      *(LAS f32x4*)(kx + 1024) = kacc[2];
      ARRIVE(E_KX);
      fxn_v = probe(fxn_p);
      const int e = hm ? 1 + (l15 >> 3) : 0, lg = 3 - e, r8 = hm ? (l15 & 7) : l15;
      const int idx = (e ? 128 + 128 * e : 0) + ((r8 >> lg) << (7 - e)) + (r8 & ((1 << lg) - 1)) + ((4 * h) << lg);
      const int id2 = min(512 + 16 * l15 + 4 * h + 2 * hm, 642);
      LAS char* kl = S + KLIN + cc * KLS;
      AWAIT(E_KX);
      LAS char* ko_ = opq(S + KX + (q4 ^ 1) * 2048 + l * 16);
      f32x4 ka = (hm ? kacc[1] : kacc[0]) + *(LAS f32x4*)(ko_);
      f32x4 kb = kacc[2] + *(LAS f32x4*)(ko_ + 1024);
      float k2a = hm ? kb[2] : kb[0], k2b = hm ? kb[3] : kb[1];
      if (gmax > 0.f) {
#pragma unroll
        for (int r = 0; r < 4; ++r) ka[r] *= __builtin_amdgcn_exp2f(gmax * (float)(((idx + (r << lg) - 128 * e) << e) - 2048));
        k2a *= __builtin_amdgcn_exp2f(gmax * (float)(((id2 - 384) << 3) - 2048));
        k2b *= __builtin_amdgcn_exp2f(gmax * (float)(((id2 - 383) << 3) - 2048));
      }
#pragma unroll
      for (int r = 0; r < 4; ++r) *(LAS _Float16*)(kl + (idx + (r << lg)) * 2) = (_Float16)ka[r];
      *(LAS u32*)(kl + id2 * 2) = pkh(k2a, k2b);
    }
    ARRIVE(E_KLIN);
    const int tc = hm * 64 + l;
    wait_flags(fxn_p, fxn_v, tag);
    u32x2 us[4];
    const u32 yo = (ch * 2048 + 16 * l15 + 4 * h) * 2;
    const u32 t0 = hm ? 512u : 0u, t1 = hm ? 1024u : 1536u, t2 = hm ? 2560u : 2048u, t3 = hm ? 3072u : 3584u;
    u32x4 ua, ub; u32 uc;
    {
      const u32 uo = WS_XNT + (ch * 2048 + tc * 16) * 2;
      ua = __builtin_amdgcn_raw_buffer_load_b128(rs_ws, uo, 0, 16);
      ub = __builtin_amdgcn_raw_buffer_load_b128(rs_ws, uo + 16, 0, 16);
      uc = __builtin_amdgcn_raw_buffer_load_b32(rs_ws, uo + 32, 0, 16);
      us[0] = __builtin_amdgcn_raw_buffer_load_b64(rs_ws, yo, WS_XNT + t0, 16);
      us[1] = __builtin_amdgcn_raw_buffer_load_b64(rs_ws, yo, WS_XNT + t1, 16);
      us[2] = __builtin_amdgcn_raw_buffer_load_b64(rs_ws, yo, WS_XNT + t2, 16);
      us[3] = __builtin_amdgcn_raw_buffer_load_b64(rs_ws, yo, WS_XNT + t3, 16);
    }
    AWAIT(E_KLIN);
    {
      LAS char* kl = S + KLIN + cc * KLS;
      const float jf = (float)(2047 - 16 * tc);
      const bool low = tc >= 120;
      u32 ko[8];
#pragma unroll
      for (int q = 0; q < 8; ++q) {
        const u32 bits = pkbf(jf - (float)(2 * q), jf - (float)(2 * q + 1));
        const u32 j0 = 2047 - 16 * tc - 2 * q;
        const u32 pr = low ? (j0 | ((j0 - 1) << 16)) : bits - 0x42804280u;
        ko[q] = (u32)*(LAS u16*)(kl + (pr & 0xffff) * 2) | ((u32)*(LAS u16*)(kl + (pr >> 16) * 2) << 16);
      }
      LAS char* kr = opq(S + KRO + cc * KRS + tc * 32);
      *(LAS u32x4*)(kr) = u32x4{ko[0], ko[1], ko[2], ko[3]};
      *(LAS u32x4*)(kr + 16) = u32x4{ko[4], ko[5], ko[6], ko[7]};
      *(LAS u32x4*)(S + KRO + cc * KRS + 4096 + min(tc, 29) * 16) = u32x4{0, 0, 0, 0};
    }
    {
      u32 d[9], o[8];
#pragma unroll
      for (int q = 0; q < 4; ++q) { d[q] = pkh(bflo(ua[q]), bfhi(ua[q])); d[4 + q] = pkh(bflo(ub[q]), bfhi(ub[q])); }
      d[8] = pkh(bflo(uc), 0.f);
#pragma unroll
      for (int q = 0; q < 8; ++q) o[q] = __builtin_amdgcn_alignbit(d[q + 1], d[q], 16);
      LAS char* ucb = opq(S + UC + cc * 2 * UCS + (2 * tc + 2) * 16);
#pragma unroll
      for (int k = 0; k < 2; ++k) {
        *(LAS u32x4*)(ucb + k * 16) = u32x4{d[4 * k], d[4 * k + 1], d[4 * k + 2], d[4 * k + 3]};
        *(LAS u32x4*)(ucb + k * 16 + UCS) = u32x4{o[4 * k], o[4 * k + 1], o[4 * k + 2], o[4 * k + 3]};
      }
      if (tc == 0) {
        LAS char* z = ucb - 32;
        *(LAS u32x4*)(z) = u32x4{0, 0, 0, 0};
        *(LAS u32x4*)(z + UCS) = u32x4{0, 0, 0, 0};
        *(LAS u32x4*)(z + 16) = u32x4{0, 0, 0, 0};
        *(LAS u32x4*)(z + UCS + 16) = u32x4{0, 0, 0, d[0] << 16};
      }
    }
    ARRIVE(E_UC);
    AWAIT(E_UC);
    {
#pragma unroll
      for (int q = 0; q < 4; ++q) ca[q] = f32x4{0, 0, 0, 0};
      const int v = l15 + 1, sft = v & 1;
      LAS char* ub0 = S + UC + (cc * 2 + sft) * UCS + (8 * h + v - sft) * 2;
      LAS char* ub[4];
#pragma unroll
      for (int q = 0; q < 4; ++q) ub[q] = opq(ub0 + q * 1024 - 64);
      LAS char* kbase = opq(S + KRO + cc * KRS + (2032 - 1792 - 16 * l15 + 8 * h) * 2);
      auto cv = [&](auto Gc) {
        constexpr int G = CV(Gc);
        constexpr int TA = G ? 1 : 0, TB = G ? 2 : 3, TC = G ? 5 : 4, TD = G ? 6 : 7;
        u32x4 U[2][TD + 1], Kf[2][TD + 1];
        auto ldu = [&](int i, int r) {
          LAS u32* p = (LAS u32*)(ub[i >> 1] + (i & 1) * 512 + r * 64);
          return u32x4{p[0], p[1], p[2], p[3]};
        };
#pragma unroll
        for (int i = 0; i <= TD; ++i) { U[0][i] = ldu(i, 1); Kf[0][TD - i] = *(LAS u32x4*)(kbase + (7 - (TD - i)) * 512); }
#pragma unroll 1
        for (int rp = 0; rp < 4; ++rp) {
          static_for<0, 2>([&](auto pc) {
            constexpr int p = CV(pc);
            static_for<0, TD + 1>([&](auto ic) {
              constexpr int i = CV(ic);
              SB();
              ca[3] = mfma_h(U[p][i], Kf[p][TD - i], ca[3]);
              SB();
              U[p ^ 1][i] = ldu(i, p + 2);
              SB();
              if constexpr (i >= TD - TC) ca[2] = mfma_h(U[p][i - (TD - TC)], Kf[p][TD - i], ca[2]);
              SB();
              Kf[p ^ 1][TD - i] = *(LAS u32x4*)(kbase + (p + 1) * 64 + (7 - (TD - i)) * 512);
              SB();
              if constexpr (i >= TD - TB) ca[1] = mfma_h(U[p][i - (TD - TB)], Kf[p][TD - i], ca[1]);
              if constexpr (i >= TD - TA) ca[0] = mfma_h(U[p][i - (TD - TA)], Kf[p][TD - i], ca[0]);
            });
          });
          SB();
#pragma unroll
          for (int q = 0; q < 4; ++q) ub[q] = opq(ub[q] + 128);
          kbase = opq(kbase + 128);
        }
      };
      if (hm == 0) cv(std::integral_constant<int, 0>{}); else cv(std::integral_constant<int, 1>{});
    }
    const float dsk = cc ? bfhi(dsk2) : bflo(dsk2);
    u32 tsk[4][2];
#pragma unroll
    for (int q = 0; q < 4; ++q) {
      tsk[q][0] = pkbf(bflo(us[q][0]) * dsk, bfhi(us[q][0]) * dsk);
      tsk[q][1] = pkbf(bflo(us[q][1]) * dsk, bfhi(us[q][1]) * dsk);
    }
    ARRIVE(E_CONV);
#pragma unroll
    for (int q = 0; q < 4; ++q) {
      const f32x4 a = ca[q];
      u32 y01 = pkbf(a[0] * yscale, a[1] * yscale), y23 = pkbf(a[2] * yscale, a[3] * yscale);
      u32x2 o = {pkbf(bflo(y01) + bflo(tsk[q][0]), bfhi(y01) + bfhi(tsk[q][0])), pkbf(bflo(y23) + bflo(tsk[q][1]), bfhi(y23) + bfhi(tsk[q][1]))};
      __builtin_amdgcn_raw_buffer_store_b64(o, rs_ws, yo, WS_YT + (q == 0 ? t0 : q == 1 ? t1 : q == 2 ? t2 : t3), 16);
    }
    AWAIT(E_W2);
    w2_regs();
    WAITV(0);
    if (l == 0) __hip_atomic_store((GAS u64*)(ws + WS_FY + (b * 4 + q4) * FSTR), tag, RLX_AGENT);
    if (b == 0 && w == 7) {
      GAS u64* p = (GAS u64*)(ws + WS_FXN + (size_t)l * 4 * FSTR);
      const u64 v0 = probe(p), v1 = probe(p + 64 * 4 * FSTR / 8), v2 = probe(p + 128 * 4 * FSTR / 8), v3 = probe(p + 192 * 4 * FSTR / 8);
      wait_flags(p, v0, tag); wait_flags(p + 64 * 4 * FSTR / 8, v1, tag);
      wait_flags(p + 128 * 4 * FSTR / 8, v2, tag); wait_flags(p + 192 * 4 * FSTR / 8, v3, tag);
      if (l == 0) __hip_atomic_store((GAS u64*)ws, tag, RLX_AGENT);
    }
    __builtin_amdgcn_s_setprio(0);
  }

  {
    GAS u64* fact_p = (GAS u64*)(ws + WS_FACT + (((bp >> 1) * 16 + 2 * (w | (l & 4)) + (l & 1)) * 8 + 2 * (bp & 1) + ((l >> 1) & 1)) * FSTR);
    wait_flags(fact_p, probe(fact_p), tag);
    const u32 ao = WS_ACT + (4 * bp * 56 + 7 * w) * 1024 + l * 16;
    u32x4 af[4][4];
#define KORD(i) ((i) < 2 ? (i) : (i) < 4 ? (i) + 2 : (i) < 6 ? (i) - 2 : 6)
#define KPOS(s) ((s) < 7 ? KORD(s) : 28 + KORD((s) - 4))
    auto lda = [&](auto sc, auto lc) {
      af[CV(sc) & 3][CV(lc)] = __builtin_amdgcn_raw_buffer_load_b128(rs_ws, ao, (CV(lc) * 56 + KPOS(CV(sc))) * 1024, 16);
    };
    static_for<0, 3>([&](auto sc) { static_for<0, 4>([&](auto lc) { lda(sc, lc); }); });
    f32x4 ac[4][4];
#pragma unroll
    for (int dt = 0; dt < 4; ++dt)
#pragma unroll
      for (int lt = 0; lt < 4; ++lt) ac[dt][lt] = f32x4{0, 0, 0, 0};
    const int yT = bp >> 2;
    GAS u64* fy_p = (GAS u64*)(ws + WS_FY + ((32 * bc + 8 * q4 + ((l >> 1) & 7)) * 4 + (l & 1) * 2 + ((yT ^ (yT >> 1)) & 1)) * FSTR);
    u64 fy_v = 0;
    u32x4 xres = __builtin_amdgcn_raw_buffer_load_b128(rs_x, (64 * bp + (t >> 3)) * 1024 + bc * 128 + (t & 7) * 16, 0, 0);
    auto step = [&](auto sc, auto nc) {
      constexpr int s = CV(sc), NS = CV(nc);
      u32x4 wf[4];
#pragma unroll
      for (int dt = 0; dt < 4; ++dt) {
        if constexpr (s < 4) wf[dt] = wr1[s][dt];
        else if constexpr (s < 7) wf[dt] = w2_frag(dt, s - 4);
        else if constexpr (s == 7) wf[dt] = w2_frag(dt, 3, 16384);
        else if constexpr (s == 8) wf[dt] = w2_frag(dt, 3);
        else wf[dt] = w2_frag(dt, s - 9);
      }
      static_for<0, 4>([&](auto lc) {
        constexpr int lt = CV(lc);
        SB();
#pragma unroll
        for (int dt = 0; dt < 4; ++dt) ac[dt][lt] = mfma_bf(wf[dt], af[s & 3][lt], ac[dt][lt]);
        SB();
        if constexpr (s == 4) ISSUE_W2P(3, lt, 0);
        if constexpr (s == 5) ISSUE_W2P(6, lt, 1);
        if constexpr (s + 3 < NS) lda(std::integral_constant<int, s + 3>{}, lc);
      });
    };
    if (w < 4) {
      static_for<0, 11>([&](auto sc) { step(sc, std::integral_constant<int, 11>{}); });
    } else {
      fy_v = probe(fy_p);
      static_for<0, 3>([&](auto sc) { step(sc, std::integral_constant<int, 3>{}); });
    }
    u32x4 y0, y1;
    if (w >= 4) {
      wait_flags(fy_p, fy_v, tag);
      const u32 yoff = WS_YT + ((64 * bc + 16 * q4 + (l >> 3)) * 2048 + 64 * bp + (l & 7) * 8) * 2;
      y0 = __builtin_amdgcn_raw_buffer_load_b128(rs_ws, yoff, 0, 16);
      y1 = __builtin_amdgcn_raw_buffer_load_b128(rs_ws, yoff, 8 * 4096, 16);
    }
    if (w < 4) ARRIVE(E_G2);
    AWAIT(E_CONV);
#pragma unroll
    for (int dt = 0; dt < 4; ++dt) {
      const int sg = ((2 * dt + (h >> 1)) & 3) * 4;
      if (dt == 3 && w >= 4) AWAIT(E_G2);
      LAS char* pw = opq(S + dt * STAGE + 4096 * w + h * 256 + ((l15 ^ sg) * 16));
#pragma unroll
      for (int lt = 0; lt < 4; ++lt) *(LAS f32x4*)(pw + lt * 1024) = ac[dt][lt];
    }
    if (w >= 4) {
      LAS char* yw = opq(S + YI + (16 * q4 + (l >> 3)) * 128 + (((l & 7) ^ (2 * q4)) * 16));
      *(LAS u32x4*)(yw) = y0;
      *(LAS u32x4*)((LAS char*)((size_t)yw ^ 16) + 1024) = y1;
    }
    BAR();
    {
      const int lrow = t >> 3, g = t & 7;
      LAS char* pr = opq(S + (g >> 1) * STAGE + (lrow >> 4) * 1024 + (2 * (g & 1)) * 256 + (((lrow & 15) ^ ((g & 3) * 4)) * 16));
      LAS char* yr = opq(S + YI + 8 * g * 128 + (((lrow >> 3) ^ g) * 16) + (lrow & 7) * 2);
      f32x4 s0 = *(LAS f32x4*)(pr), s1 = *(LAS f32x4*)(pr + 256);
#pragma unroll
      for (int p = 1; p < 8; ++p) {
        s0 += *(LAS f32x4*)(pr + 4096 * p);
        s1 += *(LAS f32x4*)(pr + 4096 * p + 256);
      }
      float yb[8] = {s0[0], s0[1], s0[2], s0[3], s1[0], s1[1], s1[2], s1[3]};
      u32 o[4];
#pragma unroll
      for (int q = 0; q < 4; ++q) {
        u32 y0 = *(LAS u16*)(yr + (2 * q) * 128);
        u32 y1 = *(LAS u16*)(yr + (2 * q + 1) * 128);
        u32 a = pkbf(bflo(xres[q]) + bflo(y0), bfhi(xres[q]) + bflo(y1));
        u32 bq = pkbf(yb[2 * q], yb[2 * q + 1]);
        o[q] = pkbf(bflo(a) + bflo(bq), bfhi(a) + bfhi(bq));
      }
      __builtin_amdgcn_raw_buffer_store_b128(u32x4{o[0], o[1], o[2], o[3]}, rs_out, (64 * bp + lrow) * 1024 + bc * 128 + g * 16, 0, 0);
    }
  }
  STAMP(11);
}

#if defined(__HIP_DEVICE_COMPILE__)
#pragma clang attribute pop
#endif

extern "C" void kernel_launch(void* const* d_in, const int* in_sizes, int n_in, void* d_out, int out_size, void* d_ws,
                              size_t ws_size, hipStream_t stream) {
  Params P;
  P.x = (const u16*)d_in[0];
  P.Are = (const u16*)d_in[1];
  P.Aim = (const u16*)d_in[2];
  P.Bre = (const u16*)d_in[3];
  P.Bim = (const u16*)d_in[4];
  P.Cre = (const u16*)d_in[5];
  P.Cim = (const u16*)d_in[6];
  P.logdt = (const u16*)d_in[7];
  P.dskip = (const u16*)d_in[8];
  P.w1 = (const u16*)d_in[10];
  P.b1 = (const u16*)d_in[11];
  P.w2 = (const u16*)d_in[12];
  P.out = (u16*)d_out;
  P.ws = (char*)d_ws;
  (void)hipFuncSetAttribute((const void*)fused_kernel, hipFuncAttributeMaxDynamicSharedMemorySize, LDS_BYTES);
  fused_kernel<<<256, 512, LDS_BYTES, stream>>>(P);
}
```

```cpp
#if defined(__HIP_DEVICE_COMPILE__)
#pragma clang attribute push(__attribute__((target("no-packed-fp32-ops"))), apply_to = function)
#endif
#include <hip/hip_runtime.h>
#include <stdint.h>
#include <type_traits>

typedef unsigned short u16;
typedef unsigned int u32;
typedef unsigned long long u64;
typedef short s16x4 __attribute__((ext_vector_type(4)));
typedef _Float16 h16x2 __attribute__((ext_vector_type(2)));
typedef _Float16 h16x8 __attribute__((ext_vector_type(8)));
typedef __bf16 b16x2 __attribute__((ext_vector_type(2)));
typedef __bf16 b16x8 __attribute__((ext_vector_type(8)));
typedef float f32x2 __attribute__((ext_vector_type(2)));
typedef float f32x4 __attribute__((ext_vector_type(4)));
typedef u32 u32x2 __attribute__((ext_vector_type(2)));
typedef u32 u32x4 __attribute__((ext_vector_type(4)));
typedef int i32x4 __attribute__((ext_vector_type(4)));
#define LAS __attribute__((address_space(3)))
#define GAS __attribute__((address_space(1)))
#define RLX_AGENT __ATOMIC_RELAXED, __HIP_MEMORY_SCOPE_AGENT

constexpr int L_ = 2048, D_ = 512, N_ = 128, F_ = 1792;
#define CONV_SPLIT 0
#define S_PRIO 0
#define POLL_SLEEP 8
#ifndef M_PRIO_E1
#define M_PRIO_E1 2
#endif
#define PRETOUCH 0
#define CHUNK 0
#define S2_BY_S 1
#define ROT 0
#define CODE_PF_BYTES 24576

constexpr u32 WS_STAT = 4096;
constexpr u32 FSTR = 128;
constexpr u32 WS_FXN = 32768;
constexpr u32 WS_FACT = WS_FXN + 1024 * FSTR;
constexpr u32 WS_FY = WS_FACT + 2048 * FSTR;
constexpr u32 WS_XNT = 1048576;
constexpr u32 WS_ACT = WS_XNT + 2097152;
constexpr u32 WS_YT = WS_ACT + 7340032;
constexpr u32 WS_END = WS_YT + 2097152;
#define DBG_OFF (16u << 20)

constexpr u32 STAGE = 32768;
constexpr u32 KT = 131072;
constexpr u32 KTW = 4352;
constexpr u32 XT = 148480;
constexpr u32 KX = 148480;
constexpr u32 KRO = 148480;
constexpr u32 KRS = 4576;
constexpr u32 DUM = 157632;
constexpr u32 SYN = 158656;
constexpr u32 CSO = 158784;
constexpr u32 STO = 129024;
constexpr u32 B1O = 129536;
constexpr u32 KLIN = 161248;
constexpr u32 KLS = 1296;
constexpr u32 UC = 131072;
constexpr u32 UCS = 4160;
constexpr u32 CRED = 131584;
constexpr u32 YI = 131584;
constexpr u32 LDS_BYTES = 163840;
enum { C_LANDED = 0, E_XT = 16, E_XTR, E_XNST, E_KX, E_KLIN, E_UC, E_CONV, E_YST, E_CS, E_ACT, E_CRED, E_W2, E_G2 };

constexpr int SPIN_CAP = 1 << 16;

__device__ __forceinline__ float bflo(u32 v) { return __uint_as_float(v << 16); }
__device__ __forceinline__ float bfhi(u32 v) { return __uint_as_float(v & 0xffff0000u); }
__device__ __forceinline__ u32 pkbf(float a, float b) {
  f32x2 v = {a, b};
  return __builtin_bit_cast(u32, __builtin_convertvector(v, b16x2));
}
__device__ __forceinline__ u32 pkh(float a, float b) {
  f32x2 v = {a, b};
  return __builtin_bit_cast(u32, __builtin_convertvector(v, h16x2));
}
__device__ __forceinline__ float rbf(float a) { return bflo(pkbf(a, 0.f)); }

__device__ __forceinline__ i32x4 mk_srd(const void* p, u32 bytes) {
  u64 a = (u64)p;
  i32x4 r;
  r[0] = (int)(u32)a;
  r[1] = (int)((u32)(a >> 32) & 0xffffu);
  r[2] = (int)bytes;
  r[3] = 0x00020000;
  return r;
}
__device__ __forceinline__ void dma16(i32x4 srd, u32 voff, u32 soff, u32 dst) {
  asm volatile("s_mov_b32 m0, %3\n\ts_nop 0\n\tbuffer_load_dwordx4 %0, %1, %2 offen lds"
               :: "v"(voff), "s"(srd), "s"(__builtin_amdgcn_readfirstlane(soff)), "s"(__builtin_amdgcn_readfirstlane(dst)));
}
__device__ __forceinline__ void dma4(i32x4 srd, u32 voff, u32 dst) {
  asm volatile("s_mov_b32 m0, %2\n\ts_nop 0\n\tbuffer_load_dword %0, %1, 0 offen lds"
               :: "v"(voff), "s"(srd), "s"(__builtin_amdgcn_readfirstlane(dst)));
}
__device__ __forceinline__ u32 ald4(i32x4 srd, u32 voff) {
  u32 r;
  asm volatile("buffer_load_dword %0, %1, %2, 0 offen" : "=v"(r) : "v"(voff), "s"(srd) : "memory");
  return r;
}
__device__ __forceinline__ u32x2 ald8(i32x4 srd, u32 voff) {
  u32x2 r;
  asm volatile("buffer_load_dwordx2 %0, %1, %2, 0 offen" : "=v"(r) : "v"(voff), "s"(srd) : "memory");
  return r;
}
__device__ __forceinline__ u32x4 ald16(i32x4 srd, u32 voff) {
  u32x4 r;
  asm volatile("buffer_load_dwordx4 %0, %1, %2, 0 offen" : "=v"(r) : "v"(voff), "s"(srd) : "memory");
  return r;
}
#define BAR() asm volatile("s_waitcnt lgkmcnt(0)\n\ts_barrier" ::: "memory")
#define WAITV(n) asm volatile("s_waitcnt vmcnt(" #n ")" ::: "memory")
#define SB() __builtin_amdgcn_sched_barrier(0)

__device__ __forceinline__ f32x4 mfma_bf(u32x4 a, u32x4 b, f32x4 c) {
  return __builtin_amdgcn_mfma_f32_16x16x32_bf16(__builtin_bit_cast(b16x8, a), __builtin_bit_cast(b16x8, b), c, 0, 0, 0);
}
__device__ __forceinline__ f32x4 mfma_h(u32x4 a, u32x4 b, f32x4 c) {
  return __builtin_amdgcn_mfma_f32_16x16x32_f16(__builtin_bit_cast(h16x8, a), __builtin_bit_cast(h16x8, b), c, 0, 0, 0);
}
__device__ __forceinline__ u32x2 tr_rd(LAS char* p) {
  return __builtin_bit_cast(u32x2, __builtin_amdgcn_ds_read_tr16_b64_v4i16((LAS s16x4*)p));
}
__device__ __forceinline__ u32x4 cat(u32x2 a, u32x2 b) { return u32x4{a[0], a[1], b[0], b[1]}; }
template <int CTRL> __device__ __forceinline__ float dpp_mov(float v) {
  return __builtin_bit_cast(float, __builtin_amdgcn_update_dpp(0, __builtin_bit_cast(int, v), CTRL, 0xf, 0xf, true));
}
__device__ __forceinline__ float rdlane(float v, int i) {
  return __builtin_bit_cast(float, __builtin_amdgcn_readlane(__builtin_bit_cast(int, v), i));
}
__device__ __forceinline__ float wave_max(float v) {
  v = fmaxf(v, dpp_mov<0xB1>(v));
  v = fmaxf(v, dpp_mov<0x4E>(v));
  v = fmaxf(v, dpp_mov<0x141>(v));
  v = fmaxf(v, dpp_mov<0x140>(v));
  return fmaxf(fmaxf(rdlane(v, 0), rdlane(v, 16)), fmaxf(rdlane(v, 32), rdlane(v, 48)));
}
__device__ __forceinline__ float wave_sum(float v) {
  v += dpp_mov<0xB1>(v);
  v += dpp_mov<0x4E>(v);
  v += dpp_mov<0x141>(v);
  v += dpp_mov<0x140>(v);
  return (rdlane(v, 0) + rdlane(v, 16)) + (rdlane(v, 32) + rdlane(v, 48));
}
__device__ __forceinline__ u64 probe(GAS u64* p) { return __hip_atomic_load(p, RLX_AGENT); }
__device__ __forceinline__ void wait_flags(GAS u64* p, u64 v, u64 tag) {
  if (!__all(v == tag)) {
#pragma unroll 1
    for (int it = 0; it < SPIN_CAP; ++it) {
      __builtin_amdgcn_s_sleep(POLL_SLEEP);
      v = __hip_atomic_load(p, RLX_AGENT);
      if (__all(v == tag)) break;
    }
  }
  __builtin_amdgcn_fence(__ATOMIC_ACQUIRE, "wavefront");
}
template <int I, int N, class F> __device__ __forceinline__ void static_for(F&& f) {
  if constexpr (I < N) { f(std::integral_constant<int, I>{}); static_for<I + 1, N>(f); }
}
#define CV(x) decltype(x)::value
__device__ __forceinline__ void lds_arrive(u32 addr) {
  u32 one = 1;
  asm volatile("s_mov_b64 exec, 1\n\tds_add_u32 %0, %1\n\ts_mov_b64 exec, -1" :: "v"(addr), "v"(one) : "memory");
}
__device__ __forceinline__ void lds_wait(LAS char* p, u32 target) {
#pragma unroll 1
  for (int it = 0; it < (1 << 22); ++it) {
    u32 v = *(volatile LAS u32*)p;
    if ((u32)__builtin_amdgcn_readfirstlane(v) >= target) break;
    __builtin_amdgcn_s_sleep(1);
  }
  asm volatile("" ::: "memory");
}
__device__ __forceinline__ void lds_spin(LAS char* p, u32 target) {
#pragma unroll 1
  for (int it = 0; it < (1 << 24); ++it) {
    u32 v = *(volatile LAS u32*)p;
    if ((u32)__builtin_amdgcn_readfirstlane(v) >= target) break;
  }
  asm volatile("" ::: "memory");
}
__device__ __forceinline__ LAS char* opq(LAS char* p) { asm volatile("" : "+v"(p)); return p; }
__device__ __forceinline__ float half_sum(float v) {
  v += dpp_mov<0xB1>(v);
  v += dpp_mov<0x4E>(v);
  v += dpp_mov<0x141>(v);
  v += dpp_mov<0x140>(v);
  auto r = __builtin_amdgcn_permlane16_swap(__builtin_bit_cast(u32, v), __builtin_bit_cast(u32, v), false, false);
  return __builtin_bit_cast(float, (u32)r[0]) + __builtin_bit_cast(float, (u32)r[1]);
}
struct cpx { float re, im; };
__device__ __forceinline__ cpx cmul(cpx a, cpx b) {
  cpx r;
  r.re = fmaf(a.re, b.re, -(a.im * b.im));
  r.im = fmaf(a.re, b.im, a.im * b.re);
  return r;
}

#define STAMP(k) do { if (t == 0) { u64 t_ = __builtin_amdgcn_s_memrealtime(); *(volatile GAS u64*)((GAS char*)ws + DBG_OFF + (b * 16 + (k)) * 8) = t_; u64 c_ = __builtin_amdgcn_s_memtime(); *(volatile GAS u64*)((GAS char*)ws + DBG_OFF + 65536 + (b * 16 + (k)) * 8) = c_; } } while (0)

struct Params {
  const u16 *x, *Are, *Aim, *Bre, *Bim, *Cre, *Cim, *logdt, *dskip, *w1, *b1, *w2;
  u16* out;
  char* ws;
};

__global__ void __launch_bounds__(512, 2) fused_kernel(Params P) {
  extern __shared__ __attribute__((aligned(16))) char smem[];
  LAS char* S = (LAS char*)smem;
  const u32 sbase = (u32)(size_t)S;
  char* ws = P.ws;
  const int t = __builtin_amdgcn_workitem_id_x(), l = t & 63, w = __builtin_amdgcn_readfirstlane(t >> 6);
  const int h = l >> 4, l15 = l & 15;
  const int b = __builtin_amdgcn_workgroup_id_x(), xcd = b & 7, rk = b >> 3;
  const int bi = 4 * (xcd >> 1) + (rk >> 3), bj = 8 * (xcd & 1) + (rk & 7);
  const int bp = 4 * xcd + (rk >> 3), bc = rk & 7;
  const int q4 = w & 3;
  STAMP(0);
  if (w == 7) dma16(mk_srd((const void*)__builtin_amdgcn_s_getpc(), CODE_PF_BYTES), rk * 1024 + l * 16, 0, sbase + DUM);

  const __amdgpu_buffer_rsrc_t rs_ws = __builtin_amdgcn_make_buffer_rsrc((void*)ws, 0, WS_END, 0x00020000);
  const __amdgpu_buffer_rsrc_t rs_x = __builtin_amdgcn_make_buffer_rsrc((void*)P.x, 0, L_ * D_ * 2, 0x00020000);
  const __amdgpu_buffer_rsrc_t rs_out = __builtin_amdgcn_make_buffer_rsrc((void*)P.out, 0, L_ * D_ * 2, 0x00020000);
  const i32x4 sx = mk_srd(P.x, L_ * D_ * 2), sw1 = mk_srd(P.w1, D_ * F_ * 2), sw2 = mk_srd(P.w2, D_ * F_ * 2);
  u64 tag;
  u32 ldt2, dsk2;
  asm volatile("s_load_dwordx2 %0, %3, 0x0\n\ts_load_dword %1, %4, 0x0\n\ts_load_dword %2, %5, 0x0"
               : "=&s"(tag), "=&s"(ldt2), "=&s"(dsk2) : "s"(ws), "s"(P.logdt + 2 * b), "s"(P.dskip + 2 * b));

  u32 p_ar, p_ai, p_br, p_bi, p_cr, p_ci;
  u32x4 sr0, sr1;
  const int cc = q4 >> 1, hm = q4 & 1, ch = 2 * b + cc;
  if (w < 4) {
    if (t < 32) *(LAS u32*)(S + SYN + t * 4) = 0;
  } else {
    const u32 o = (128 * bi + 8 * bj + 2 * q4) * 1024 + l * 32;
    sr0 = ald16(sx, o);
    sr1 = ald16(sx, o + 16);
    const u32 po = ch * 256 + 4 * l;
    p_ar = ald4(mk_srd(P.Are, D_ * N_ * 2), po);
    p_ai = ald4(mk_srd(P.Aim, D_ * N_ * 2), po);
    p_br = ald4(mk_srd(P.Bre, D_ * N_ * 2), po);
    p_bi = ald4(mk_srd(P.Bim, D_ * N_ * 2), po);
    p_cr = ald4(mk_srd(P.Cre, D_ * N_ * 2), po);
    p_ci = ald4(mk_srd(P.Cim, D_ * N_ * 2), po);
  }
  const u32 dq = q4 < 3 ? 4096u : 12288u, dq2 = q4 < 3 ? 11264u : 12288u;
  const u32 vx = (128 * bi + 8 * q4 + (l >> 3)) * 1024 + (((l & 7) ^ (l >> 3)) * 16);
  u32 vwa, vwb;
  {
    const u32 g0 = q4 * 64 + l, pos0 = (g0 * 4682u) >> 16, c0 = g0 - pos0 * 14;
    const u32 g1 = g0 + 256, pos1 = (g1 * 4682u) >> 16, c1 = g1 - pos1 * 14;
    const u32 k0 = (pos0 & ~12u) | ((pos0 & 4) << 1) | ((pos0 & 8) >> 1);
    const u32 k1 = (pos1 & ~12u) | ((pos1 & 4) << 1) | ((pos1 & 8) >> 1);
    vwa = k0 * (F_ * 2) + bj * 224 + c0 * 16;
    vwb = q4 < 3 ? k1 * (F_ * 2) + bj * 224 + c1 * 16 : 0x7ff00000u;
  }
  auto issue_stage = [&](int c) {
    const u32 sb_ = sbase + c * STAGE + q4 * 1024;
#pragma unroll
    for (int i = 0; i < 4; ++i) dma16(sx, vx, c * 128 + i * 32768, sb_ + i * 4096);
    const u32 so = c * (64 * F_ * 2);
    dma16(sw1, vwa, so, sb_ + 16384);
    dma16(sw1, vwa, so + 32 * F_ * 2, sb_ + 16384 + 7168);
    dma16(sw1, vwb, so, sb_ + 16384 + dq);
    dma16(sw1, vwb, so + 32 * F_ * 2, sb_ + 16384 + dq2);
  };
  if (w < 4) issue_stage(0);
  BAR();
  asm volatile("" : "+s"(tag), "+s"(ldt2), "+s"(dsk2));
  tag += 1ull;
  issue_stage(1 + (w >> 2));
  u32 w2o;
  LAS char* w2a[4];
  {
    const int rr = l >> 3, slot = l & 7;
    const int dt = (slot >> 1) ^ ((rr >> 1) & 3);
    w2o = (224 * w + rr) * 1024 + bc * 128 + (2 * dt + (slot & 1)) * 16;
    const int q = l15 >> 2, p = l & 3, x = (2 * h + (q >> 1)) & 3;
#pragma unroll
    for (int d = 0; d < 4; ++d) w2a[d] = S + 4096 * w + (4 * h + q) * 128 + ((d ^ x) * 32) + (p >> 1) * 16 + (p & 1) * 8;
  }
#define W2TILE(ks, hf) ((ks) < 3 ? 2 * (ks) + (hf) : ((ks) == 3 ? ((hf) ? 13 : 6) : 7 + 2 * ((ks) - 4) + (hf)))
#define ISSUE_W2(p, n, slot) dma16(sw2, w2o, (W2TILE(p, (n) >> 1) * 16 + ((n) & 1) * 8) * 1024, sbase + (slot) * STAGE + 4096 * w + (n) * 1024)
#define ISSUE_W2P(p, n, slot) dma16(sw2, w2o, (W2TILE(p, (n) >> 1) * 16 + ((n) & 1) * 8 + 896) * 1024, sbase + (slot) * STAGE + 4096 * w + (n) * 1024)
  auto w2_frag = [&](int d, int slot, int extra = 0) {
    LAS char* p = (slot >= 2 ? opq(w2a[d] + 2 * STAGE) + (slot - 2) * STAGE : w2a[d] + slot * STAGE) + extra;
    return cat(tr_rd(p), tr_rd(p + 2048));
  };
  u32x4 wr1[4][4];
  auto w2_regs = [&]() {
#pragma unroll
    for (int k = 0; k < 4; ++k)
#pragma unroll
      for (int d = 0; d < 4; ++d) wr1[k][d] = w2_frag(d, k);
    asm volatile("s_waitcnt lgkmcnt(0)" ::: "memory");
  };
  auto w2_req = [&]() {
#pragma unroll
    for (int n = 0; n < 4; ++n) ISSUE_W2(2, n, 0);
#pragma unroll
    for (int n = 0; n < 4; ++n) ISSUE_W2(3, n, 1);
#pragma unroll
    for (int n = 0; n < 4; ++n) ISSUE_W2(6, n, 2);
#pragma unroll
    for (int n = 0; n < 4; ++n) ISSUE_W2P(2, n, 3);
  };
  f32x4 ca[4];
  const u32 syn = sbase + SYN;
  LAS char* synp = opq(S + SYN);
#define ARRIVE(e) lds_arrive(syn + (e) * 4)
#define AWAIT(e) lds_wait(synp + (e) * 4, 4)

  if (w < 4) {
    f32x4 acc[7][2], cs0 = {0, 0, 0, 0}, cs1 = {0, 0, 0, 0};
#pragma unroll
    for (int f = 0; f < 7; ++f) { acc[f][0] = f32x4{0, 0, 0, 0}; acc[f][1] = f32x4{0, 0, 0, 0}; }
    u32 xa = (32 * q4 + l15) * 128 + ((h ^ (l & 7)) * 16);
    u32 wo = 16384 + (16 * (h >> 1) + 4 * (h & 1) + (l15 >> 2)) * 224 + (l & 3) * 8;
    u32x4 ones = {0x3f803f80u, 0x3f803f80u, 0x3f803f80u, 0x3f803f80u};
    asm volatile("" : "+v"(ones));
    u32x4 xf[2][2];
    u32x2 wl[2][7], wh[2][7];
    u32 lnd = 0;
    auto kstep = [&](auto kc, auto mc, u32 so, u32 dst, u32 cnt_addr, LAS char* cnt_p, u32 tgt, int nwait) {
      constexpr int k = CV(kc), n = k ^ 1, MODE = CV(mc);
      constexpr int r0 = k ? 4 : 0;
      LAS char* xp = S + (k ? xa : (xa ^ 64));
      LAS char* wp = S + wo + (k ? 0 : 32 * 224);
      static_for<0, 14>([&](auto jc) {
        constexpr int j = CV(jc), f = j >> 1, mt = j & 1;
        SB();
        acc[f][mt] = mfma_bf(cat(wl[k][f], wh[k][f]), xf[k][mt], acc[f][mt]);
        SB();
        if constexpr (k == 1 && j == 1) lnd = *(volatile LAS u32*)cnt_p;
        if constexpr (k == 1 && j == 3) { if ((u32)__builtin_amdgcn_readfirstlane(lnd) < tgt) lds_spin(cnt_p, tgt); }
        if constexpr (j == r0) { xf[n][0] = *(LAS u32x4*)(xp); xf[n][1] = *(LAS u32x4*)(xp + 2048); }
        if constexpr (j >= r0 + 1 && j <= r0 + 7) {
          wl[n][j - r0 - 1] = tr_rd(wp + (j - r0 - 1) * 32);
          wh[n][j - r0 - 1] = tr_rd(wp + 8 * 224 + (j - r0 - 1) * 32);
        }
        constexpr int pc = j == (k ? 5 : 1) ? 0 : j == (k ? 8 : 5) ? 1 : j == (k ? 11 : 10) ? 2 : j == (k ? 13 : 12) ? 3 : -1;
        if constexpr (pc >= 0 && MODE == 1 && k == 0)
          dma16(sw1, pc < 2 ? vwa : vwb, so + (pc & 1) * (32 * F_ * 2), dst + (pc == 0 ? 0u : pc == 1 ? 7168u : pc == 2 ? dq : dq2));
        if constexpr (pc >= 0 && MODE == 1 && k == 1) dma16(sx, vx, so + pc * 32768, dst + pc * 4096);
        if constexpr (pc >= 0 && MODE == 2) dma16(sw2, w2o, so + pc * 8192, dst + pc * 1024);
        if constexpr (k == 0 && j == 8) {
          if (nwait == 2) WAITV(2); else if (nwait) WAITV(10);
          lds_arrive(cnt_addr);
        }
      });
      SB();
    };
    auto colsum = [&](auto kc) {
      constexpr int k = CV(kc);
      if (q4 < 2) {
        if (q4 == 0) { cs0 = mfma_bf(cat(wl[k][0], wh[k][0]), ones, cs0); cs1 = mfma_bf(cat(wl[k][4], wh[k][4]), ones, cs1); }
        else { cs0 = mfma_bf(cat(wl[k][1], wh[k][1]), ones, cs0); cs1 = mfma_bf(cat(wl[k][5], wh[k][5]), ones, cs1); }
      } else {
        if (q4 == 2) { cs0 = mfma_bf(cat(wl[k][2], wh[k][2]), ones, cs0); cs1 = mfma_bf(cat(wl[k][6], wh[k][6]), ones, cs1); }
        else cs0 = mfma_bf(cat(wl[k][3], wh[k][3]), ones, cs0);
      }
    };
    using I0 = std::integral_constant<int, 0>; using I1 = std::integral_constant<int, 1>; using I2 = std::integral_constant<int, 2>;
    WAITV(8);
    ARRIVE(C_LANDED);
    lds_spin(synp + C_LANDED * 4, 4);
    xf[0][0] = *(LAS u32x4*)(S + xa); xf[0][1] = *(LAS u32x4*)(S + xa + 2048);
#pragma unroll
    for (int f = 0; f < 7; ++f) { wl[0][f] = tr_rd(S + wo + f * 32); wh[0][f] = tr_rd(S + wo + 8 * 224 + f * 32); }
    auto adv = [&]() { xa = (xa + STAGE) & 131071u; wo = (wo + STAGE) & 131071u; };
#pragma unroll 1
    for (int c = 0; c < 5; ++c) {
      const u32 sl = sbase + ((c + 3) & 3) * STAGE + q4 * 1024;
      kstep(I0{}, I1{}, (c + 3) * (64 * F_ * 2), sl + 16384, syn + (c + 1) * 4, synp, 0, (S2_BY_S && c == 0) ? 2 : 10);
      colsum(I0{});
      adv();
      kstep(I1{}, I1{}, (c + 3) * 128, sl, 0, synp + (c + 1) * 4, (S2_BY_S && c == 1) ? 8u : 4u, 0);
      colsum(I1{});
    }
#pragma unroll 1
    for (int c = 5; c < 8; ++c) {
      const u32 sl = sbase + (c - 5) * STAGE + 4096 * w, wso = c < 7 ? (c - 5) * 32768 : 7 * 16384;
      kstep(I0{}, I2{}, wso, sl, syn + (c + 1) * 4, synp, 0, c < 7);
      colsum(I0{});
      adv();
      kstep(I1{}, I2{}, wso + 896 * 1024, sl + 16384, 0, synp + (c + 1) * 4, c < 7 ? 4u : 0u, 0);
      colsum(I1{});
    }
    if (l15 == 0) {
      LAS char* cp = opq(S + CSO + q4 * 64 + h * 16);
      *(LAS f32x4*)(cp) = cs0;
      *(LAS f32x4*)(cp + 256) = cs1;
    }
    AWAIT(E_XT);
    u32 pay2[2];
    u32x2 b1v[7];
    {
      LAS char* sp = opq(S + STO + (32 * q4 + l15) * 4);
      LAS char* bp_ = opq(S + B1O + h * 8);
      pay2[0] = *(LAS u32*)sp; pay2[1] = *(LAS u32*)(sp + 64);
#pragma unroll
      for (int f = 0; f < 7; ++f) b1v[f] = *(LAS u32x2*)(bp_ + 32 * f);
    }
    ARRIVE(E_CS);
    const float LOG2E = 1.4426950408889634f, LN2 = 0.6931471805599453f;
    float rs[2], tm[2];
#pragma unroll
    for (int mt = 0; mt < 2; ++mt) {
      rs[mt] = __builtin_amdgcn_rcpf(bfhi(pay2[mt])) * LOG2E;
      tm[mt] = -bflo(pay2[mt]) * rs[mt];
    }
    lds_spin(synp + E_CS * 4, 4);
    __builtin_amdgcn_s_setprio(M_PRIO_E1);
    const int jp = bj & 1;
    const u32 abase = WS_ACT + ((8 * bi + 2 * q4) * 56 + 7 * (bj >> 1) + (jp ? 4 : 0)) * 1024 + l * 16;
    LAS char* csp = opq(S + CSO + h * 16);
    u32x2 pk[2][2];
    static_for<0, 7>([&](auto fc) {
      constexpr int f = CV(fc);
      const f32x4 cs = *(LAS f32x4*)(csp + 64 * f);
      const float bia[4] = {bflo(b1v[f][0]) * LOG2E, bfhi(b1v[f][0]) * LOG2E, bflo(b1v[f][1]) * LOG2E, bfhi(b1v[f][1]) * LOG2E};
#pragma unroll
      for (int mt = 0; mt < 2; ++mt) {
        float a[4];
#pragma unroll
        for (int r = 0; r < 4; ++r) {
          float g = fmaf(acc[f][mt][r], rs[mt], fmaf(tm[mt], cs[r], bia[r]));
          float s1 = __builtin_amdgcn_exp2f(g) + 1.f;
          float rr = __builtin_amdgcn_rcpf(fmaf(s1, s1, 1.f));
          a[r] = g * fmaf(rr, -2.f * LN2, LN2);
        }
        pk[f & 1][mt] = u32x2{pkbf(a[0], a[1]), pkbf(a[2], a[3])};
        if constexpr (f < 2) {
          SB();
          const int n = 2 * f + mt;
          const u32 so = 9 * 16384 + n * 8192, dd = sbase + 3 * STAGE + 4096 * w + n * 1024;
          dma16(sw2, w2o, so, dd); dma16(sw2, w2o, so + 896 * 1024, dd + 16384);
          SB();
        }
      }
      if constexpr (f & 1) {
#pragma unroll
        for (int mt = 0; mt < 2; ++mt)
          __builtin_amdgcn_raw_buffer_store_b128(cat(pk[0][mt], pk[1][mt]), rs_ws, abase + (f >> 1) * 1024, mt * 56 * 1024, 16);
      }
      if constexpr (f == 6) {
#pragma unroll
        for (int mt = 0; mt < 2; ++mt)
          __builtin_amdgcn_raw_buffer_store_b64(pk[0][mt], rs_ws, abase + (jp ? -1024 + 8 : 3072), mt * 56 * 1024, 16);
      }
    });
    WAITV(6);
    ARRIVE(E_W2);
    w2_regs();
    WAITV(0);
    if (l == 0) __hip_atomic_store((GAS u64*)(ws + WS_FACT + ((bi * 16 + bj) * 8 + q4) * FSTR), tag, RLX_AGENT);
    __builtin_amdgcn_s_setprio(0);
    w2_req();
  } else {
    const int ts = t - 256;
    __builtin_amdgcn_s_setprio(S_PRIO);
    WAITV(8);
    asm volatile("" : "+v"(p_ar), "+v"(p_ai), "+v"(p_br), "+v"(p_bi), "+v"(p_cr), "+v"(p_ci));
    asm volatile("" : "+v"(sr0), "+v"(sr1));
    {
      float e[16];
#pragma unroll
      for (int q = 0; q < 4; ++q) { e[2 * q] = bflo(sr0[q]); e[2 * q + 1] = bfhi(sr0[q]); e[8 + 2 * q] = bflo(sr1[q]); e[9 + 2 * q] = bfhi(sr1[q]); }
      float s0 = (e[0] + e[1]) + (e[2] + e[3]), s1 = (e[4] + e[5]) + (e[6] + e[7]), s2 = (e[8] + e[9]) + (e[10] + e[11]), s3 = (e[12] + e[13]) + (e[14] + e[15]);
      const float mean = half_sum((s0 + s1) + (s2 + s3)) * (1.0f / D_);
      float v0 = 0, v1 = 0;
#pragma unroll
      for (int q = 0; q < 8; ++q) { float d0 = e[q] - mean, d1 = e[8 + q] - mean; v0 = fmaf(d0, d0, v0); v1 = fmaf(d1, d1, v1); }
      const float var = half_sum(v0 + v1) * (1.0f / D_);
      const float sd = __builtin_amdgcn_sqrtf(rbf(rbf(var) + 1e-5f));
      const u32 pay = pkbf(mean, sd);
      if ((l & 31) == 0) __hip_atomic_store((GAS u64*)(ws + WS_STAT + (128 * bi + 8 * bj + 2 * q4 + (l >> 5)) * 8), ((u64)(u32)tag << 32) | pay, RLX_AGENT);
    }
    WAITV(1);
    ARRIVE(C_LANDED + 2);
    u32x4 xs0 = ald16(sx, (128 * bi + (ts >> 1)) * 1024 + bj * 64 + (ts & 1) * 32);
    u32x4 xs1 = ald16(sx, (128 * bi + (ts >> 1)) * 1024 + bj * 64 + (ts & 1) * 32 + 16);
    u32 b1w = ald4(mk_srd(P.b1, F_ * 2), bj * 224 + 4 * l);
    float yscale, gmax, zr, zi;
    cpx cbn;
    {
      const float LOG2E = 1.4426950408889634f, INV2PI = 0.15915494309189535f;
      const float lx = cc ? bfhi(ldt2) : bflo(ldt2);
      const float th = lx * LOG2E, tl = fmaf(lx, LOG2E, -th) + lx * 1.925963033500011e-8f;
      const float dt = rbf(__builtin_amdgcn_exp2f(th) * fmaf(tl, 0.6931471805599453f, 1.0f));
      cpx cb0 = cmul(cpx{bflo(p_cr), bflo(p_ci)}, cpx{dt * bflo(p_br), dt * bflo(p_bi)});
      cpx cb1 = cmul(cpx{bfhi(p_cr), bfhi(p_ci)}, cpx{dt * bfhi(p_br), dt * bfhi(p_bi)});
      const float z0 = dt * bflo(p_ar) * LOG2E, z1 = dt * bfhi(p_ar) * LOG2E;
      gmax = fmaxf(wave_max(fmaxf(z0, z1)), 0.f);
      const float cbmax = wave_max(fmaxf(fmaxf(fabsf(cb0.re), fabsf(cb0.im)), fmaxf(fabsf(cb1.re), fabsf(cb1.im))));
      const float cbs = __uint_as_float((__float_as_uint(cbmax) + 0x007fffffu) & 0x7f800000u);
      const float icb = cbs > 0.f ? __builtin_amdgcn_rcpf(cbs) : 0.f;
      yscale = cbs * __builtin_amdgcn_exp2f(gmax * 2048.f) * (1.0f / 64.f);
      zr = fmaxf((hm ? z1 : z0) - gmax, -1e30f);
      zi = dt * (hm ? bfhi(p_ai) : bflo(p_ai)) * INV2PI;
      cbn = hm ? cb1 : cb0;
      cbn.re *= 64.f * icb; cbn.im *= -64.f * icb;
    }
    auto cexp = [&](float a) {
      float mg = __builtin_amdgcn_exp2f(zr * a);
      float ph = __builtin_amdgcn_fractf(zi * a);
      return cpx{mg * __builtin_amdgcn_cosf(ph), mg * __builtin_amdgcn_sinf(ph)};
    };
    const cpx e1 = cexp(1.f), e8 = cexp(8.f), e128 = cexp(128.f);
    GAS u64* stat_p = (GAS u64*)(ws + WS_STAT + (128 * bi + (ts >> 1)) * 8);
    u64 stat_v = 0;
    LAS char* tb = opq(S + KT + q4 * KTW + l * 4);
    LAS char* tf = opq(S + KT + q4 * KTW + l15 * 272 + h * 16);
    auto put = [&](int row, cpx v) { *(volatile LAS u32*)(tb + row * 272) = pkh(v.re, v.im); };
    u32x4 fa[4];
    f32x4 kacc[3];
    auto tile_mma = [&]() {
      f32x4 a = {0, 0, 0, 0};
#pragma unroll
      for (int k = 0; k < 4; ++k) a = mfma_h(fa[k], *(volatile LAS u32x4*)(tf + k * 64), a);
      return a;
    };
    {
      cpx wv = cbn;
      const cpx e8c = cpx{e8.re, -e8.im};
#pragma unroll
      for (int q = 0; q < 16; ++q) { put(q, wv); if (q < 15) wv = cmul(wv, e8c); if (q == 6) stat_v = probe(stat_p); }
#pragma unroll
      for (int k = 0; k < 4; ++k) fa[k] = *(volatile LAS u32x4*)(tf + k * 64);
    }
    if (!__all((u32)(stat_v >> 32) == (u32)tag)) {
#pragma unroll 1
      for (int it = 0; it < SPIN_CAP; ++it) {
        __builtin_amdgcn_s_sleep(16);
        stat_v = probe(stat_p);
        if (__all((u32)(stat_v >> 32) == (u32)tag)) break;
      }
    }
    WAITV(0);
    asm volatile("" : "+v"(xs0), "+v"(xs1), "+v"(b1w));
    {
      const u32 pay = (u32)stat_v;
      const float mb = bflo(pay), rsd = __builtin_amdgcn_rcpf(bfhi(pay));
      *(LAS u32*)(S + STO + (ts >> 1) * 4) = pay;
      *(LAS u32*)(S + B1O + l * 4) = b1w;
      LAS char* xt = opq(S + XT + (ts & 1) * 16 * 272 + (ts >> 1) * 2);
#pragma unroll
      for (int q = 0; q < 8; ++q) {
        const u32 xv = q < 4 ? xs0[q] : xs1[q - 4];
        u32 dd = pkbf(bflo(xv) - mb, bfhi(xv) - mb);
        u32 xn = pkbf(bflo(dd) * rsd, bfhi(dd) * rsd);
        *(LAS u16*)(xt + (2 * q) * 272) = (u16)xn;
        *(LAS u16*)(xt + (2 * q + 1) * 272) = (u16)(xn >> 16);
      }
      ARRIVE(E_XT);
    }
    cpx e2, e4, e6;
    {
      cpx lo = cpx{1.f, 0.f}, g = e128;
#pragma unroll
      for (int q = 0; q < 8; ++q) {
        put(q, lo); put(8 + q, g);
        if (q == 2) e2 = lo;
        if (q == 4) e4 = lo;
        if (q == 6) e6 = lo;
        if (q < 7) { lo = (q == 0) ? e1 : cmul(lo, e1); g = cmul(g, e1); }
        if (q == 1) {
          AWAIT(E_XT);
          LAS char* xr = opq(S + XT + (ts >> 4) * 272 + (ts & 15) * 16);
          u32x4 v0 = *(LAS u32x4*)(xr), v1 = *(LAS u32x4*)(xr + 16 * 272);
          const u32 xo = WS_XNT + ((32 * bj + (ts >> 4)) * 2048 + 128 * bi + (ts & 15) * 8) * 2;
          __builtin_amdgcn_raw_buffer_store_b128(v0, rs_ws, xo, 0, 16);
          __builtin_amdgcn_raw_buffer_store_b128(v1, rs_ws, xo, 16 * 4096, 16);
          ARRIVE(E_XTR);
        }
      }
      kacc[0] = tile_mma();
    }
    cpx hv = cmul(e128, e128);
#pragma unroll
    for (int a = 2; a < 8; ++a) {
      if (a < 4) {
        put((a - 2) * 4 + 0, hv); put((a - 2) * 4 + 1, cmul(hv, e2));
        put((a - 2) * 4 + 2, cmul(hv, e4)); put((a - 2) * 4 + 3, cmul(hv, e6));
      } else {
        put(8 + (a - 4) * 2 + 0, hv); put(8 + (a - 4) * 2 + 1, cmul(hv, e4));
      }
      hv = cmul(hv, e128);
      if (a == 3) {
        WAITV(0);
        if (l == 0) __hip_atomic_store((GAS u64*)(ws + WS_FXN + ((bj * 16 + bi) * 4 + q4) * FSTR), tag, RLX_AGENT);
      }
    }
    kacc[1] = tile_mma();
#pragma unroll
    for (int a = 8; a <= 16; ++a) { put(a - 8, hv); if (a < 16) hv = cmul(hv, e128); }
    GAS u64* fxn_p = (GAS u64*)(ws + WS_FXN + (((ch >> 5) * 16 + l15) * 4 + ((ch & 15) >> 2)) * FSTR);
    u64 fxn_v;
    kacc[2] = tile_mma();
    AWAIT(E_XTR);
    {
      LAS char* kx = opq(S + KX + q4 * 2048 + l * 16);
      *(LAS f32x4*)(kx) = hm ? kacc[0] : kacc[1];
      *(LAS f32x4*)(kx + 1024) = kacc[2];
      ARRIVE(E_KX);
      fxn_v = probe(fxn_p);
      const int e = hm ? 1 + (l15 >> 3) : 0, lg = 3 - e, r8 = hm ? (l15 & 7) : l15;
      const int idx = (e ? 128 + 128 * e : 0) + ((r8 >> lg) << (7 - e)) + (r8 & ((1 << lg) - 1)) + ((4 * h) << lg);
      const int id2 = min(512 + 16 * l15 + 4 * h + 2 * hm, 642);
      LAS char* kl = S + KLIN + cc * KLS;
      AWAIT(E_KX);
      LAS char* ko_ = opq(S + KX + (q4 ^ 1) * 2048 + l * 16);
      f32x4 ka = (hm ? kacc[1] : kacc[0]) + *(LAS f32x4*)(ko_);
      f32x4 kb = kacc[2] + *(LAS f32x4*)(ko_ + 1024);
      float k2a = hm ? kb[2] : kb[0], k2b = hm ? kb[3] : kb[1];
      if (gmax > 0.f) {
#pragma unroll
        for (int r = 0; r < 4; ++r) ka[r] *= __builtin_amdgcn_exp2f(gmax * (float)(((idx + (r << lg) - 128 * e) << e) - 2048));
        k2a *= __builtin_amdgcn_exp2f(gmax * (float)(((id2 - 384) << 3) - 2048));
        k2b *= __builtin_amdgcn_exp2f(gmax * (float)(((id2 - 383) << 3) - 2048));
      }
#pragma unroll
      for (int r = 0; r < 4; ++r) *(LAS _Float16*)(kl + (idx + (r << lg)) * 2) = (_Float16)ka[r];
      *(LAS u32*)(kl + id2 * 2) = pkh(k2a, k2b);
    }
    ARRIVE(E_KLIN);
    const int tc = hm * 64 + l;
    wait_flags(fxn_p, fxn_v, tag);
    u32x2 us[4];
    const u32 yo = (ch * 2048 + 16 * l15 + 4 * h) * 2;
    const u32 t0 = hm ? 512u : 0u, t1 = hm ? 1024u : 1536u, t2 = hm ? 2560u : 2048u, t3 = hm ? 3072u : 3584u;
    u32x4 ua, ub; u32 uc;
    {
      const u32 uo = WS_XNT + (ch * 2048 + tc * 16) * 2;
      ua = __builtin_amdgcn_raw_buffer_load_b128(rs_ws, uo, 0, 16);
      ub = __builtin_amdgcn_raw_buffer_load_b128(rs_ws, uo + 16, 0, 16);
      uc = __builtin_amdgcn_raw_buffer_load_b32(rs_ws, uo + 32, 0, 16);
      us[0] = __builtin_amdgcn_raw_buffer_load_b64(rs_ws, yo, WS_XNT + t0, 16);
      us[1] = __builtin_amdgcn_raw_buffer_load_b64(rs_ws, yo, WS_XNT + t1, 16);
      us[2] = __builtin_amdgcn_raw_buffer_load_b64(rs_ws, yo, WS_XNT + t2, 16);
      us[3] = __builtin_amdgcn_raw_buffer_load_b64(rs_ws, yo, WS_XNT + t3, 16);
    }
    AWAIT(E_KLIN);
    {
      LAS char* kl = S + KLIN + cc * KLS;
      const float jf = (float)(2047 - 16 * tc);
      const bool low = tc >= 120;
      u32 ko[8];
#pragma unroll
      for (int q = 0; q < 8; ++q) {
        const u32 bits = pkbf(jf - (float)(2 * q), jf - (float)(2 * q + 1));
        const u32 j0 = 2047 - 16 * tc - 2 * q;
        const u32 pr = low ? (j0 | ((j0 - 1) << 16)) : bits - 0x42804280u;
        ko[q] = (u32)*(LAS u16*)(kl + (pr & 0xffff) * 2) | ((u32)*(LAS u16*)(kl + (pr >> 16) * 2) << 16);
      }
      LAS char* kr = opq(S + KRO + cc * KRS + tc * 32);
      *(LAS u32x4*)(kr) = u32x4{ko[0], ko[1], ko[2], ko[3]};
      *(LAS u32x4*)(kr + 16) = u32x4{ko[4], ko[5], ko[6], ko[7]};
      *(LAS u32x4*)(S + KRO + cc * KRS + 4096 + min(tc, 29) * 16) = u32x4{0, 0, 0, 0};
    }
    {
      u32 d[9], o[8];
#pragma unroll
      for (int q = 0; q < 4; ++q) { d[q] = pkh(bflo(ua[q]), bfhi(ua[q])); d[4 + q] = pkh(bflo(ub[q]), bfhi(ub[q])); }
      d[8] = pkh(bflo(uc), 0.f);
#pragma unroll
      for (int q = 0; q < 8; ++q) o[q] = __builtin_amdgcn_alignbit(d[q + 1], d[q], 16);
      LAS char* ucb = opq(S + UC + cc * 2 * UCS + (2 * tc + 2) * 16);
#pragma unroll
      for (int k = 0; k < 2; ++k) {
        *(LAS u32x4*)(ucb + k * 16) = u32x4{d[4 * k], d[4 * k + 1], d[4 * k + 2], d[4 * k + 3]};
        *(LAS u32x4*)(ucb + k * 16 + UCS) = u32x4{o[4 * k], o[4 * k + 1], o[4 * k + 2], o[4 * k + 3]};
      }
      if (tc == 0) {
        LAS char* z = ucb - 32;
        *(LAS u32x4*)(z) = u32x4{0, 0, 0, 0};
        *(LAS u32x4*)(z + UCS) = u32x4{0, 0, 0, 0};
        *(LAS u32x4*)(z + 16) = u32x4{0, 0, 0, 0};
        *(LAS u32x4*)(z + UCS + 16) = u32x4{0, 0, 0, d[0] << 16};
      }
    }
    ARRIVE(E_UC);
    AWAIT(E_UC);
    {
#pragma unroll
      for (int q = 0; q < 4; ++q) ca[q] = f32x4{0, 0, 0, 0};
      const int v = l15 + 1, sft = v & 1;
      LAS char* ub0 = S + UC + (cc * 2 + sft) * UCS + (8 * h + v - sft) * 2;
      LAS char* ub[4];
#pragma unroll
      for (int q = 0; q < 4; ++q) ub[q] = opq(ub0 + q * 1024 - 64);
      LAS char* kbase = opq(S + KRO + cc * KRS + (2032 - 1792 - 16 * l15 + 8 * h) * 2);
      auto cv = [&](auto Gc) {
        constexpr int G = CV(Gc);
        constexpr int TA = G ? 1 : 0, TB = G ? 2 : 3, TC = G ? 5 : 4, TD = G ? 6 : 7;
        u32x4 U[2][TD + 1], Kf[2][TD + 1];
        auto ldu = [&](int i, int r) {
          LAS u32* p = (LAS u32*)(ub[i >> 1] + (i & 1) * 512 + r * 64);
          return u32x4{p[0], p[1], p[2], p[3]};
        };
#pragma unroll
        for (int i = 0; i <= TD; ++i) { U[0][i] = ldu(i, 1); Kf[0][TD - i] = *(LAS u32x4*)(kbase + (7 - (TD - i)) * 512); }
#pragma unroll 1
        for (int rp = 0; rp < 4; ++rp) {
          static_for<0, 2>([&](auto pc) {
            constexpr int p = CV(pc);
            static_for<0, TD + 1>([&](auto ic) {
              constexpr int i = CV(ic);
              SB();
              ca[3] = mfma_h(U[p][i], Kf[p][TD - i], ca[3]);
              SB();
              U[p ^ 1][i] = ldu(i, p + 2);
              SB();
              if constexpr (i >= TD - TC) ca[2] = mfma_h(U[p][i - (TD - TC)], Kf[p][TD - i], ca[2]);
              SB();
              Kf[p ^ 1][TD - i] = *(LAS u32x4*)(kbase + (p + 1) * 64 + (7 - (TD - i)) * 512);
              SB();
              if constexpr (i >= TD - TB) ca[1] = mfma_h(U[p][i - (TD - TB)], Kf[p][TD - i], ca[1]);
              if constexpr (i >= TD - TA) ca[0] = mfma_h(U[p][i - (TD - TA)], Kf[p][TD - i], ca[0]);
            });
          });
          SB();
#pragma unroll
          for (int q = 0; q < 4; ++q) ub[q] = opq(ub[q] + 128);
          kbase = opq(kbase + 128);
        }
      };
      if (hm == 0) cv(std::integral_constant<int, 0>{}); else cv(std::integral_constant<int, 1>{});
    }
    const float dsk = cc ? bfhi(dsk2) : bflo(dsk2);
    u32 tsk[4][2];
#pragma unroll
    for (int q = 0; q < 4; ++q) {
      tsk[q][0] = pkbf(bflo(us[q][0]) * dsk, bfhi(us[q][0]) * dsk);
      tsk[q][1] = pkbf(bflo(us[q][1]) * dsk, bfhi(us[q][1]) * dsk);
    }
    ARRIVE(E_CONV);
#pragma unroll
    for (int q = 0; q < 4; ++q) {
      const f32x4 a = ca[q];
      u32 y01 = pkbf(a[0] * yscale, a[1] * yscale), y23 = pkbf(a[2] * yscale, a[3] * yscale);
      u32x2 o = {pkbf(bflo(y01) + bflo(tsk[q][0]), bfhi(y01) + bfhi(tsk[q][0])), pkbf(bflo(y23) + bflo(tsk[q][1]), bfhi(y23) + bfhi(tsk[q][1]))};
      __builtin_amdgcn_raw_buffer_store_b64(o, rs_ws, yo, WS_YT + (q == 0 ? t0 : q == 1 ? t1 : q == 2 ? t2 : t3), 16);
    }
    AWAIT(E_W2);
    w2_regs();
    WAITV(0);
    if (l == 0) __hip_atomic_store((GAS u64*)(ws + WS_FY + (b * 4 + q4) * FSTR), tag, RLX_AGENT);
    if (b == 0 && w == 7) {
      GAS u64* p = (GAS u64*)(ws + WS_FXN + (size_t)l * 4 * FSTR);
      const u64 v0 = probe(p), v1 = probe(p + 64 * 4 * FSTR / 8), v2 = probe(p + 128 * 4 * FSTR / 8), v3 = probe(p + 192 * 4 * FSTR / 8);
      wait_flags(p, v0, tag); wait_flags(p + 64 * 4 * FSTR / 8, v1, tag);
      wait_flags(p + 128 * 4 * FSTR / 8, v2, tag); wait_flags(p + 192 * 4 * FSTR / 8, v3, tag);
      if (l == 0) __hip_atomic_store((GAS u64*)ws, tag, RLX_AGENT);
    }
    __builtin_amdgcn_s_setprio(0);
  }

  {
    GAS u64* fact_p = (GAS u64*)(ws + WS_FACT + (((bp >> 1) * 16 + 2 * (w | (l & 4)) + (l & 1)) * 8 + 2 * (bp & 1) + ((l >> 1) & 1)) * FSTR);
    wait_flags(fact_p, probe(fact_p), tag);
    const u32 ao = WS_ACT + (4 * bp * 56 + 7 * w) * 1024 + l * 16;
    u32x4 af[4][4];
#define KORD(i) ((i) < 2 ? (i) : (i) < 4 ? (i) + 2 : (i) < 6 ? (i) - 2 : 6)
#define KPOS(s) ((s) < 7 ? KORD(s) : 28 + KORD((s) - 4))
    auto lda = [&](auto sc, auto lc) {
      af[CV(sc) & 3][CV(lc)] = __builtin_amdgcn_raw_buffer_load_b128(rs_ws, ao, (CV(lc) * 56 + KPOS(CV(sc))) * 1024, 16);
    };
    static_for<0, 3>([&](auto sc) { static_for<0, 4>([&](auto lc) { lda(sc, lc); }); });
    f32x4 ac[4][4];
#pragma unroll
    for (int dt = 0; dt < 4; ++dt)
#pragma unroll
      for (int lt = 0; lt < 4; ++lt) ac[dt][lt] = f32x4{0, 0, 0, 0};
    const int yT = bp >> 2;
    GAS u64* fy_p = (GAS u64*)(ws + WS_FY + ((32 * bc + 8 * q4 + ((l >> 1) & 7)) * 4 + (l & 1) * 2 + ((yT ^ (yT >> 1)) & 1)) * FSTR);
    u64 fy_v = 0;
    u32x4 xres = __builtin_amdgcn_raw_buffer_load_b128(rs_x, (64 * bp + (t >> 3)) * 1024 + bc * 128 + (t & 7) * 16, 0, 0);
    auto step = [&](auto sc, auto nc) {
      constexpr int s = CV(sc), NS = CV(nc);
      u32x4 wf[4];
#pragma unroll
      for (int dt = 0; dt < 4; ++dt) {
        if constexpr (s < 4) wf[dt] = wr1[s][dt];
        else if constexpr (s < 7) wf[dt] = w2_frag(dt, s - 4);
        else if constexpr (s == 7) wf[dt] = w2_frag(dt, 3, 16384);
        else if constexpr (s == 8) wf[dt] = w2_frag(dt, 3);
        else wf[dt] = w2_frag(dt, s - 9);
      }
      static_for<0, 4>([&](auto lc) {
        constexpr int lt = CV(lc);
        SB();
#pragma unroll
        for (int dt = 0; dt < 4; ++dt) ac[dt][lt] = mfma_bf(wf[dt], af[s & 3][lt], ac[dt][lt]);
        SB();
        if constexpr (s == 4) ISSUE_W2P(3, lt, 0);
        if constexpr (s == 5) ISSUE_W2P(6, lt, 1);
        if constexpr (s + 3 < NS) lda(std::integral_constant<int, s + 3>{}, lc);
      });
    };
    if (w < 4) {
      static_for<0, 11>([&](auto sc) { step(sc, std::integral_constant<int, 11>{}); });
    } else {
      fy_v = probe(fy_p);
      static_for<0, 3>([&](auto sc) { step(sc, std::integral_constant<int, 3>{}); });
    }
    u32x4 y0, y1;
    if (w >= 4) {
      wait_flags(fy_p, fy_v, tag);
      const u32 yoff = WS_YT + ((64 * bc + 16 * q4 + (l >> 3)) * 2048 + 64 * bp + (l & 7) * 8) * 2;
      y0 = __builtin_amdgcn_raw_buffer_load_b128(rs_ws, yoff, 0, 16);
      y1 = __builtin_amdgcn_raw_buffer_load_b128(rs_ws, yoff, 8 * 4096, 16);
    }
    if (w < 4) ARRIVE(E_G2);
    AWAIT(E_CONV);
#pragma unroll
    for (int dt = 0; dt < 4; ++dt) {
      const int sg = ((2 * dt + (h >> 1)) & 3) * 4;
      if (dt == 3 && w >= 4) AWAIT(E_G2);
      LAS char* pw = opq(S + dt * STAGE + 4096 * w + h * 256 + ((l15 ^ sg) * 16));
#pragma unroll
      for (int lt = 0; lt < 4; ++lt) *(LAS f32x4*)(pw + lt * 1024) = ac[dt][lt];
    }
    if (w >= 4) {
      LAS char* yw = opq(S + YI + (16 * q4 + (l >> 3)) * 128 + (((l & 7) ^ (2 * q4)) * 16));
      *(LAS u32x4*)(yw) = y0;
      *(LAS u32x4*)((LAS char*)((size_t)yw ^ 16) + 1024) = y1;
    }
    BAR();
    {
      const int lrow = t >> 3, g = t & 7;
      LAS char* pr = opq(S + (g >> 1) * STAGE + (lrow >> 4) * 1024 + (2 * (g & 1)) * 256 + (((lrow & 15) ^ ((g & 3) * 4)) * 16));
      LAS char* yr = opq(S + YI + 8 * g * 128 + (((lrow >> 3) ^ g) * 16) + (lrow & 7) * 2);
      f32x4 s0 = *(LAS f32x4*)(pr), s1 = *(LAS f32x4*)(pr + 256);
#pragma unroll
      for (int p = 1; p < 8; ++p) {
        s0 += *(LAS f32x4*)(pr + 4096 * p);
        s1 += *(LAS f32x4*)(pr + 4096 * p + 256);
      }
      float yb[8] = {s0[0], s0[1], s0[2], s0[3], s1[0], s1[1], s1[2], s1[3]};
      u32 o[4];
#pragma unroll
      for (int q = 0; q < 4; ++q) {
        u32 y0 = *(LAS u16*)(yr + (2 * q) * 128);
        u32 y1 = *(LAS u16*)(yr + (2 * q + 1) * 128);
        u32 a = pkbf(bflo(xres[q]) + bflo(y0), bfhi(xres[q]) + bflo(y1));
        u32 bq = pkbf(yb[2 * q], yb[2 * q + 1]);
        o[q] = pkbf(bflo(a) + bflo(bq), bfhi(a) + bfhi(bq));
      }
      __builtin_amdgcn_raw_buffer_store_b128(u32x4{o[0], o[1], o[2], o[3]}, rs_out, (64 * bp + lrow) * 1024 + bc * 128 + g * 16, 0, 0);
    }
  }
  STAMP(11);
}

#if defined(__HIP_DEVICE_COMPILE__)
#pragma clang attribute pop
#endif

__global__ void dbg_print(char* ws) {
  const u64* s = (const u64*)(ws + DBG_OFF); const u64* c = (const u64*)(ws + DBG_OFF + 65536);
  u64 t0 = ~0ull, t0max = 0, e0 = ~0ull, e1 = 0;
  for (int b = 0; b < 256; ++b) { u64 a = s[b * 16], e = s[b * 16 + 11]; t0 = a < t0 ? a : t0; t0max = a > t0max ? a : t0max; e0 = e < e0 ? e : e0; e1 = e > e1 ? e : e1; }
  printf("DBG start skew %llu x10ns, end min %llu max %llu x10ns, blk0 cycles %llu over %llu x10ns\n", t0max - t0, e0 - t0, e1 - t0, c[11] - c[0], s[11] - s[0]);
}
extern "C" void kernel_launch(void* const* d_in, const int* in_sizes, int n_in, void* d_out, int out_size, void* d_ws,
                              size_t ws_size, hipStream_t stream) {
  Params P;
  P.x = (const u16*)d_in[0];
  P.Are = (const u16*)d_in[1];
  P.Aim = (const u16*)d_in[2];
  P.Bre = (const u16*)d_in[3];
  P.Bim = (const u16*)d_in[4];
  P.Cre = (const u16*)d_in[5];
  P.Cim = (const u16*)d_in[6];
  P.logdt = (const u16*)d_in[7];
  P.dskip = (const u16*)d_in[8];
  P.w1 = (const u16*)d_in[10];
  P.b1 = (const u16*)d_in[11];
  P.w2 = (const u16*)d_in[12];
  P.out = (u16*)d_out;
  P.ws = (char*)d_ws;
  (void)hipFuncSetAttribute((const void*)fused_kernel, hipFuncAttributeMaxDynamicSharedMemorySize, LDS_BYTES);
  fused_kernel<<<256, 512, LDS_BYTES, stream>>>(P);
  dbg_print<<<1, 1, 0, stream>>>((char*)d_ws);
}
```

```cpp
#if defined(__HIP_DEVICE_COMPILE__)
#pragma clang attribute push(__attribute__((target("no-packed-fp32-ops"))), apply_to = function)
#endif
#include <hip/hip_runtime.h>
#include <stdint.h>
#include <type_traits>

typedef unsigned short u16;
typedef unsigned int u32;
typedef unsigned long long u64;
typedef short s16x4 __attribute__((ext_vector_type(4)));
typedef _Float16 h16x2 __attribute__((ext_vector_type(2)));
typedef _Float16 h16x8 __attribute__((ext_vector_type(8)));
typedef __bf16 b16x2 __attribute__((ext_vector_type(2)));
typedef __bf16 b16x8 __attribute__((ext_vector_type(8)));
typedef float f32x2 __attribute__((ext_vector_type(2)));
typedef float f32x4 __attribute__((ext_vector_type(4)));
typedef u32 u32x2 __attribute__((ext_vector_type(2)));
typedef u32 u32x4 __attribute__((ext_vector_type(4)));
typedef int i32x4 __attribute__((ext_vector_type(4)));
#define LAS __attribute__((address_space(3)))
#define GAS __attribute__((address_space(1)))
#define RLX_AGENT __ATOMIC_RELAXED, __HIP_MEMORY_SCOPE_AGENT

constexpr int L_ = 2048, D_ = 512, N_ = 128, F_ = 1792;
#define S_PRIO 0
#define POLL_SLEEP 8
#define M_PRIO_E1 2
#define S2_BY_S 1
#define CODE_PF_BYTES 24576

constexpr u32 WS_STAT = 4096;
constexpr u32 FSTR = 128;
constexpr u32 WS_FXN = 32768;
constexpr u32 WS_FACT = WS_FXN + 1024 * FSTR;
constexpr u32 WS_FY = WS_FACT + 2048 * FSTR;
constexpr u32 WS_XNT = 1048576;
constexpr u32 WS_ACT = WS_XNT + 2097152;
constexpr u32 WS_YT = WS_ACT + 7340032;
constexpr u32 WS_END = WS_YT + 2097152;

constexpr u32 STAGE = 32768;
constexpr u32 KT = 131072;
constexpr u32 KTW = 4352;
constexpr u32 XT = 148480;
constexpr u32 KX = 148480;
constexpr u32 KRO = 148480;
constexpr u32 KRS = 4576;
constexpr u32 DUM = 157632;
constexpr u32 SYN = 158656;
constexpr u32 CSO = 158784;
constexpr u32 STO = 129024;
constexpr u32 B1O = 129536;
constexpr u32 KLIN = 161248;
constexpr u32 KLS = 1296;
constexpr u32 UC = 131072;
constexpr u32 UCS = 4160;
constexpr u32 CRED = 131584;
constexpr u32 YI = 131584;
constexpr u32 LDS_BYTES = 163840;
enum { C_LANDED = 0, E_XT = 16, E_XTR, E_XNST, E_KX, E_KLIN, E_UC, E_CONV, E_YST, E_CS, E_ACT, E_CRED, E_W2, E_G2 };

constexpr int SPIN_CAP = 1 << 15;

__device__ __forceinline__ float bflo(u32 v) { return __uint_as_float(v << 16); }
__device__ __forceinline__ float bfhi(u32 v) { return __uint_as_float(v & 0xffff0000u); }
__device__ __forceinline__ u32 pkbf(float a, float b) {
  f32x2 v = {a, b};
  return __builtin_bit_cast(u32, __builtin_convertvector(v, b16x2));
}
__device__ __forceinline__ u32 pkh(float a, float b) {
  f32x2 v = {a, b};
  return __builtin_bit_cast(u32, __builtin_convertvector(v, h16x2));
}
__device__ __forceinline__ float rbf(float a) { return bflo(pkbf(a, 0.f)); }

__device__ __forceinline__ i32x4 mk_srd(const void* p, u32 bytes) {
  u64 a = (u64)p;
  i32x4 r;
  r[0] = (int)(u32)a;
  r[1] = (int)((u32)(a >> 32) & 0xffffu);
  r[2] = (int)bytes;
  r[3] = 0x00020000;
  return r;
}
__device__ __forceinline__ void dma16(i32x4 srd, u32 voff, u32 soff, u32 dst) {
  asm volatile("s_mov_b32 m0, %3\n\ts_nop 0\n\tbuffer_load_dwordx4 %0, %1, %2 offen lds"
               :: "v"(voff), "s"(srd), "s"(__builtin_amdgcn_readfirstlane(soff)), "s"(__builtin_amdgcn_readfirstlane(dst)));
}
__device__ __forceinline__ void dma4(i32x4 srd, u32 voff, u32 dst) {
  asm volatile("s_mov_b32 m0, %2\n\ts_nop 0\n\tbuffer_load_dword %0, %1, 0 offen lds"
               :: "v"(voff), "s"(srd), "s"(__builtin_amdgcn_readfirstlane(dst)));
}
__device__ __forceinline__ u32 ald4(i32x4 srd, u32 voff) {
  u32 r;
  asm volatile("buffer_load_dword %0, %1, %2, 0 offen" : "=v"(r) : "v"(voff), "s"(srd) : "memory");
  return r;
}
__device__ __forceinline__ u32x2 ald8(i32x4 srd, u32 voff) {
  u32x2 r;
  asm volatile("buffer_load_dwordx2 %0, %1, %2, 0 offen" : "=v"(r) : "v"(voff), "s"(srd) : "memory");
  return r;
}
__device__ __forceinline__ u32x4 ald16(i32x4 srd, u32 voff) {
  u32x4 r;
  asm volatile("buffer_load_dwordx4 %0, %1, %2, 0 offen" : "=v"(r) : "v"(voff), "s"(srd) : "memory");
  return r;
}
#define BAR() asm volatile("s_waitcnt lgkmcnt(0)\n\ts_barrier" ::: "memory")
#define WAITV(n) asm volatile("s_waitcnt vmcnt(" #n ")" ::: "memory")
#define SB() __builtin_amdgcn_sched_barrier(0)

__device__ __forceinline__ f32x4 mfma_bf(u32x4 a, u32x4 b, f32x4 c) {
  return __builtin_amdgcn_mfma_f32_16x16x32_bf16(__builtin_bit_cast(b16x8, a), __builtin_bit_cast(b16x8, b), c, 0, 0, 0);
}
__device__ __forceinline__ f32x4 mfma_h(u32x4 a, u32x4 b, f32x4 c) {
  return __builtin_amdgcn_mfma_f32_16x16x32_f16(__builtin_bit_cast(h16x8, a), __builtin_bit_cast(h16x8, b), c, 0, 0, 0);
}
__device__ __forceinline__ u32x2 tr_rd(LAS char* p) {
  return __builtin_bit_cast(u32x2, __builtin_amdgcn_ds_read_tr16_b64_v4i16((LAS s16x4*)p));
}
__device__ __forceinline__ u32x4 cat(u32x2 a, u32x2 b) { return u32x4{a[0], a[1], b[0], b[1]}; }
template <int CTRL> __device__ __forceinline__ float dpp_mov(float v) {
  return __builtin_bit_cast(float, __builtin_amdgcn_update_dpp(0, __builtin_bit_cast(int, v), CTRL, 0xf, 0xf, true));
}
__device__ __forceinline__ float rdlane(float v, int i) {
  return __builtin_bit_cast(float, __builtin_amdgcn_readlane(__builtin_bit_cast(int, v), i));
}
__device__ __forceinline__ float wave_max(float v) {
  v = fmaxf(v, dpp_mov<0xB1>(v));
  v = fmaxf(v, dpp_mov<0x4E>(v));
  v = fmaxf(v, dpp_mov<0x141>(v));
  v = fmaxf(v, dpp_mov<0x140>(v));
  return fmaxf(fmaxf(rdlane(v, 0), rdlane(v, 16)), fmaxf(rdlane(v, 32), rdlane(v, 48)));
}
__device__ __forceinline__ float wave_sum(float v) {
  v += dpp_mov<0xB1>(v);
  v += dpp_mov<0x4E>(v);
  v += dpp_mov<0x141>(v);
  v += dpp_mov<0x140>(v);
  return (rdlane(v, 0) + rdlane(v, 16)) + (rdlane(v, 32) + rdlane(v, 48));
}
__device__ __forceinline__ u64 probe(GAS u64* p) { return __hip_atomic_load(p, RLX_AGENT); }
__device__ __forceinline__ void wait_flags(GAS u64* p, u64 v, u64 tag) {
  if (!__all(v == tag)) {
#pragma unroll 1
    for (int it = 0; it < SPIN_CAP; ++it) {
      __builtin_amdgcn_s_sleep(POLL_SLEEP);
      v = __hip_atomic_load(p, RLX_AGENT);
      if (__all(v == tag)) break;
    }
  }
  __builtin_amdgcn_fence(__ATOMIC_ACQUIRE, "wavefront");
}
template <int I, int N, class F> __device__ __forceinline__ void static_for(F&& f) {
  if constexpr (I < N) { f(std::integral_constant<int, I>{}); static_for<I + 1, N>(f); }
}
#define CV(x) decltype(x)::value
__device__ __forceinline__ void lds_arrive(u32 addr) {
  u32 one = 1;
  asm volatile("s_mov_b64 exec, 1\n\tds_add_u32 %0, %1\n\ts_mov_b64 exec, -1" :: "v"(addr), "v"(one) : "memory");
}
__device__ __forceinline__ void lds_wait(LAS char* p, u32 target) {
#pragma unroll 1
  for (int it = 0; it < (1 << 20); ++it) {
    u32 v = *(volatile LAS u32*)p;
    if ((u32)__builtin_amdgcn_readfirstlane(v) >= target) break;
    __builtin_amdgcn_s_sleep(1);
  }
  asm volatile("" ::: "memory");
}
__device__ __forceinline__ void lds_spin(LAS char* p, u32 target) {
#pragma unroll 1
  for (int it = 0; it < (1 << 22); ++it) {
    u32 v = *(volatile LAS u32*)p;
    if ((u32)__builtin_amdgcn_readfirstlane(v) >= target) break;
  }
  asm volatile("" ::: "memory");
}
__device__ __forceinline__ LAS char* opq(LAS char* p) { asm volatile("" : "+v"(p)); return p; }
__device__ __forceinline__ float half_sum(float v) {
  v += dpp_mov<0xB1>(v);
  v += dpp_mov<0x4E>(v);
  v += dpp_mov<0x141>(v);
  v += dpp_mov<0x140>(v);
  auto r = __builtin_amdgcn_permlane16_swap(__builtin_bit_cast(u32, v), __builtin_bit_cast(u32, v), false, false);
  return __builtin_bit_cast(float, (u32)r[0]) + __builtin_bit_cast(float, (u32)r[1]);
}
struct cpx { float re, im; };
__device__ __forceinline__ cpx cmul(cpx a, cpx b) {
  cpx r;
  r.re = fmaf(a.re, b.re, -(a.im * b.im));
  r.im = fmaf(a.re, b.im, a.im * b.re);
  return r;
}

struct Params {
  const u16 *x, *Are, *Aim, *Bre, *Bim, *Cre, *Cim, *logdt, *dskip, *w1, *b1, *w2;
  u16* out;
  char* ws;
};

__global__ void __launch_bounds__(512, 2) fused_kernel(Params P) {
  extern __shared__ __attribute__((aligned(16))) char smem[];
  LAS char* S = (LAS char*)smem;
  const u32 sbase = (u32)(size_t)S;
  char* ws = P.ws;
  const int t = __builtin_amdgcn_workitem_id_x(), l = t & 63, w = __builtin_amdgcn_readfirstlane(t >> 6);
  const int h = l >> 4, l15 = l & 15;
  const int b = __builtin_amdgcn_workgroup_id_x(), xcd = b & 7, rk = b >> 3;
  const int bi = 4 * (xcd >> 1) + (rk >> 3), bj = 8 * (xcd & 1) + (rk & 7);
  const int bp = 4 * xcd + (rk >> 3), bc = rk & 7;
  const int q4 = w & 3;
  if (w == 7) dma16(mk_srd((const void*)__builtin_amdgcn_s_getpc(), CODE_PF_BYTES), rk * 1024 + l * 16, 0, sbase + DUM);

  const __amdgpu_buffer_rsrc_t rs_ws = __builtin_amdgcn_make_buffer_rsrc((void*)ws, 0, WS_END, 0x00020000);
  const __amdgpu_buffer_rsrc_t rs_x = __builtin_amdgcn_make_buffer_rsrc((void*)P.x, 0, L_ * D_ * 2, 0x00020000);
  const __amdgpu_buffer_rsrc_t rs_out = __builtin_amdgcn_make_buffer_rsrc((void*)P.out, 0, L_ * D_ * 2, 0x00020000);
  const i32x4 sx = mk_srd(P.x, L_ * D_ * 2), sw1 = mk_srd(P.w1, D_ * F_ * 2), sw2 = mk_srd(P.w2, D_ * F_ * 2);
  u64 tag;
  u32 ldt2, dsk2;
  asm volatile("s_load_dwordx2 %0, %3, 0x0\n\ts_load_dword %1, %4, 0x0\n\ts_load_dword %2, %5, 0x0"
               : "=&s"(tag), "=&s"(ldt2), "=&s"(dsk2) : "s"(ws), "s"(P.logdt + 2 * b), "s"(P.dskip + 2 * b));

  u32 p_ar, p_ai, p_br, p_bi, p_cr, p_ci;
  u32x4 sr0, sr1;
  const int cc = q4 >> 1, hm = q4 & 1, ch = 2 * b + cc;
  if (w < 4) {
    if (t < 32) *(LAS u32*)(S + SYN + t * 4) = 0;
  } else {
    const u32 o = (128 * bi + 8 * bj + 2 * q4) * 1024 + l * 32;
    sr0 = ald16(sx, o);
    sr1 = ald16(sx, o + 16);
    const u32 po = ch * 256 + 4 * l;
    p_ar = ald4(mk_srd(P.Are, D_ * N_ * 2), po);
    p_ai = ald4(mk_srd(P.Aim, D_ * N_ * 2), po);
    p_br = ald4(mk_srd(P.Bre, D_ * N_ * 2), po);
    p_bi = ald4(mk_srd(P.Bim, D_ * N_ * 2), po);
    p_cr = ald4(mk_srd(P.Cre, D_ * N_ * 2), po);
    p_ci = ald4(mk_srd(P.Cim, D_ * N_ * 2), po);
  }
  const u32 dq = q4 < 3 ? 4096u : 12288u, dq2 = q4 < 3 ? 11264u : 12288u;
  const u32 vx = (128 * bi + 8 * q4 + (l >> 3)) * 1024 + (((l & 7) ^ (l >> 3)) * 16);
  u32 vwa, vwb;
  {
    const u32 g0 = q4 * 64 + l, pos0 = (g0 * 4682u) >> 16, c0 = g0 - pos0 * 14;
    const u32 g1 = g0 + 256, pos1 = (g1 * 4682u) >> 16, c1 = g1 - pos1 * 14;
    const u32 k0 = (pos0 & ~12u) | ((pos0 & 4) << 1) | ((pos0 & 8) >> 1);
    const u32 k1 = (pos1 & ~12u) | ((pos1 & 4) << 1) | ((pos1 & 8) >> 1);
    vwa = k0 * (F_ * 2) + bj * 224 + c0 * 16;
    vwb = q4 < 3 ? k1 * (F_ * 2) + bj * 224 + c1 * 16 : 0x7ff00000u;
  }
  auto issue_stage = [&](int c) {
    const u32 sb_ = sbase + c * STAGE + q4 * 1024;
#pragma unroll
    for (int i = 0; i < 4; ++i) dma16(sx, vx, c * 128 + i * 32768, sb_ + i * 4096);
    const u32 so = c * (64 * F_ * 2);
    dma16(sw1, vwa, so, sb_ + 16384);
    dma16(sw1, vwa, so + 32 * F_ * 2, sb_ + 16384 + 7168);
    dma16(sw1, vwb, so, sb_ + 16384 + dq);
    dma16(sw1, vwb, so + 32 * F_ * 2, sb_ + 16384 + dq2);
  };
  if (w < 4) issue_stage(0);
  BAR();
  asm volatile("" : "+s"(tag), "+s"(ldt2), "+s"(dsk2));
  tag += 1ull;
  issue_stage(1 + (w >> 2));
  u32 w2o;
  LAS char* w2a[4];
  {
    const int rr = l >> 3, slot = l & 7;
    const int dt = (slot >> 1) ^ ((rr >> 1) & 3);
    w2o = (224 * w + rr) * 1024 + bc * 128 + (2 * dt + (slot & 1)) * 16;
    const int q = l15 >> 2, p = l & 3, x = (2 * h + (q >> 1)) & 3;
#pragma unroll
    for (int d = 0; d < 4; ++d) w2a[d] = S + 4096 * w + (4 * h + q) * 128 + ((d ^ x) * 32) + (p >> 1) * 16 + (p & 1) * 8;
  }
#define W2TILE(ks, hf) ((ks) < 3 ? 2 * (ks) + (hf) : ((ks) == 3 ? ((hf) ? 13 : 6) : 7 + 2 * ((ks) - 4) + (hf)))
#define ISSUE_W2(p, n, slot) dma16(sw2, w2o, (W2TILE(p, (n) >> 1) * 16 + ((n) & 1) * 8) * 1024, sbase + (slot) * STAGE + 4096 * w + (n) * 1024)
#define ISSUE_W2P(p, n, slot) dma16(sw2, w2o, (W2TILE(p, (n) >> 1) * 16 + ((n) & 1) * 8 + 896) * 1024, sbase + (slot) * STAGE + 4096 * w + (n) * 1024)
  auto w2_frag = [&](int d, int slot, int extra = 0) {
    LAS char* p = (slot >= 2 ? opq(w2a[d] + 2 * STAGE) + (slot - 2) * STAGE : w2a[d] + slot * STAGE) + extra;
    return cat(tr_rd(p), tr_rd(p + 2048));
  };
  u32x4 wr1[4][4];
  auto w2_regs = [&]() {
#pragma unroll
    for (int k = 0; k < 4; ++k)
#pragma unroll
      for (int d = 0; d < 4; ++d) wr1[k][d] = w2_frag(d, k);
    asm volatile("s_waitcnt lgkmcnt(0)" ::: "memory");
  };
  auto w2_req = [&]() {
#pragma unroll
    for (int n = 0; n < 4; ++n) ISSUE_W2(2, n, 0);
#pragma unroll
    for (int n = 0; n < 4; ++n) ISSUE_W2(3, n, 1);
#pragma unroll
    for (int n = 0; n < 4; ++n) ISSUE_W2(6, n, 2);
#pragma unroll
    for (int n = 0; n < 4; ++n) ISSUE_W2P(2, n, 3);
  };
  f32x4 ca[4];
  const u32 syn = sbase + SYN;
  LAS char* synp = opq(S + SYN);
#define ARRIVE(e) lds_arrive(syn + (e) * 4)
#define AWAIT(e) lds_wait(synp + (e) * 4, 4)

  if (w < 4) {
    f32x4 acc[7][2], cs0 = {0, 0, 0, 0}, cs1 = {0, 0, 0, 0};
#pragma unroll
    for (int f = 0; f < 7; ++f) { acc[f][0] = f32x4{0, 0, 0, 0}; acc[f][1] = f32x4{0, 0, 0, 0}; }
    u32 xa = (32 * q4 + l15) * 128 + ((h ^ (l & 7)) * 16);
    u32 wo = 16384 + (16 * (h >> 1) + 4 * (h & 1) + (l15 >> 2)) * 224 + (l & 3) * 8;
    u32x4 ones = {0x3f803f80u, 0x3f803f80u, 0x3f803f80u, 0x3f803f80u};
    asm volatile("" : "+v"(ones));
    u32x4 xf[2][2];
    u32x2 wl[2][7], wh[2][7];
    u32 lnd = 0;
    auto kstep = [&](auto kc, auto mc, u32 so, u32 dst, u32 cnt_addr, LAS char* cnt_p, u32 tgt, int nwait) {
      constexpr int k = CV(kc), n = k ^ 1, MODE = CV(mc);
      constexpr int r0 = k ? 4 : 0;
      LAS char* xp = S + (k ? xa : (xa ^ 64));
      LAS char* wp = S + wo + (k ? 0 : 32 * 224);
      static_for<0, 14>([&](auto jc) {
        constexpr int j = CV(jc), f = j >> 1, mt = j & 1;
        SB();
        acc[f][mt] = mfma_bf(cat(wl[k][f], wh[k][f]), xf[k][mt], acc[f][mt]);
        SB();
        if constexpr (k == 1 && j == 1) lnd = *(volatile LAS u32*)cnt_p;
        if constexpr (k == 1 && j == 3) { if ((u32)__builtin_amdgcn_readfirstlane(lnd) < tgt) lds_spin(cnt_p, tgt); }
        if constexpr (j == r0) { xf[n][0] = *(LAS u32x4*)(xp); xf[n][1] = *(LAS u32x4*)(xp + 2048); }
        if constexpr (j >= r0 + 1 && j <= r0 + 7) {
          wl[n][j - r0 - 1] = tr_rd(wp + (j - r0 - 1) * 32);
          wh[n][j - r0 - 1] = tr_rd(wp + 8 * 224 + (j - r0 - 1) * 32);
        }
        constexpr int pc = j == (k ? 5 : 1) ? 0 : j == (k ? 8 : 5) ? 1 : j == (k ? 11 : 10) ? 2 : j == (k ? 13 : 12) ? 3 : -1;
        if constexpr (pc >= 0 && MODE == 1 && k == 0)
          dma16(sw1, pc < 2 ? vwa : vwb, so + (pc & 1) * (32 * F_ * 2), dst + (pc == 0 ? 0u : pc == 1 ? 7168u : pc == 2 ? dq : dq2));
        if constexpr (pc >= 0 && MODE == 1 && k == 1) dma16(sx, vx, so + pc * 32768, dst + pc * 4096);
        if constexpr (pc >= 0 && MODE == 2) dma16(sw2, w2o, so + pc * 8192, dst + pc * 1024);
        if constexpr (k == 0 && j == 8) {
          if (nwait == 2) WAITV(2); else if (nwait) WAITV(10);
          lds_arrive(cnt_addr);
        }
      });
      SB();
    };
    auto colsum = [&](auto kc) {
      constexpr int k = CV(kc);
      if (q4 < 2) {
        if (q4 == 0) { cs0 = mfma_bf(cat(wl[k][0], wh[k][0]), ones, cs0); cs1 = mfma_bf(cat(wl[k][4], wh[k][4]), ones, cs1); }
        else { cs0 = mfma_bf(cat(wl[k][1], wh[k][1]), ones, cs0); cs1 = mfma_bf(cat(wl[k][5], wh[k][5]), ones, cs1); }
      } else {
        if (q4 == 2) { cs0 = mfma_bf(cat(wl[k][2], wh[k][2]), ones, cs0); cs1 = mfma_bf(cat(wl[k][6], wh[k][6]), ones, cs1); }
        else cs0 = mfma_bf(cat(wl[k][3], wh[k][3]), ones, cs0);
      }
    };
    using I0 = std::integral_constant<int, 0>; using I1 = std::integral_constant<int, 1>; using I2 = std::integral_constant<int, 2>;
    WAITV(8);
    ARRIVE(C_LANDED);
    lds_spin(synp + C_LANDED * 4, 4);
    xf[0][0] = *(LAS u32x4*)(S + xa); xf[0][1] = *(LAS u32x4*)(S + xa + 2048);
#pragma unroll
    for (int f = 0; f < 7; ++f) { wl[0][f] = tr_rd(S + wo + f * 32); wh[0][f] = tr_rd(S + wo + 8 * 224 + f * 32); }
    auto adv = [&]() { xa = (xa + STAGE) & 131071u; wo = (wo + STAGE) & 131071u; };
#pragma unroll 1
    for (int c = 0; c < 5; ++c) {
      const u32 sl = sbase + ((c + 3) & 3) * STAGE + q4 * 1024;
      kstep(I0{}, I1{}, (c + 3) * (64 * F_ * 2), sl + 16384, syn + (c + 1) * 4, synp, 0, (S2_BY_S && c == 0) ? 2 : 10);
      colsum(I0{});
      adv();
      kstep(I1{}, I1{}, (c + 3) * 128, sl, 0, synp + (c + 1) * 4, (S2_BY_S && c == 1) ? 8u : 4u, 0);
      colsum(I1{});
    }
#pragma unroll 1
    for (int c = 5; c < 8; ++c) {
      const u32 sl = sbase + (c - 5) * STAGE + 4096 * w, wso = c < 7 ? (c - 5) * 32768 : 7 * 16384;
      kstep(I0{}, I2{}, wso, sl, syn + (c + 1) * 4, synp, 0, c < 7);
      colsum(I0{});
      adv();
      kstep(I1{}, I2{}, wso + 896 * 1024, sl + 16384, 0, synp + (c + 1) * 4, c < 7 ? 4u : 0u, 0);
      colsum(I1{});
    }
    if (l15 == 0) {
      LAS char* cp = opq(S + CSO + q4 * 64 + h * 16);
      *(LAS f32x4*)(cp) = cs0;
      *(LAS f32x4*)(cp + 256) = cs1;
    }
    AWAIT(E_XT);
    u32 pay2[2];
    u32x2 b1v[7];
    {
      LAS char* sp = opq(S + STO + (32 * q4 + l15) * 4);
      LAS char* bp_ = opq(S + B1O + h * 8);
      pay2[0] = *(LAS u32*)sp; pay2[1] = *(LAS u32*)(sp + 64);
#pragma unroll
      for (int f = 0; f < 7; ++f) b1v[f] = *(LAS u32x2*)(bp_ + 32 * f);
    }
    ARRIVE(E_CS);
    const float LOG2E = 1.4426950408889634f, LN2 = 0.6931471805599453f;
    float rs[2], tm[2];
#pragma unroll
    for (int mt = 0; mt < 2; ++mt) {
      rs[mt] = __builtin_amdgcn_rcpf(bfhi(pay2[mt])) * LOG2E;
      tm[mt] = -bflo(pay2[mt]) * rs[mt];
    }
    lds_spin(synp + E_CS * 4, 4);
    __builtin_amdgcn_s_setprio(M_PRIO_E1);
    const int jp = bj & 1;
    const u32 abase = WS_ACT + ((8 * bi + 2 * q4) * 56 + 7 * (bj >> 1) + (jp ? 4 : 0)) * 1024 + l * 16;
    LAS char* csp = opq(S + CSO + h * 16);
    u32x2 pk[2][2];
    static_for<0, 7>([&](auto fc) {
      constexpr int f = CV(fc);
      const f32x4 cs = *(LAS f32x4*)(csp + 64 * f);
      const float bia[4] = {bflo(b1v[f][0]) * LOG2E, bfhi(b1v[f][0]) * LOG2E, bflo(b1v[f][1]) * LOG2E, bfhi(b1v[f][1]) * LOG2E};
#pragma unroll
      for (int mt = 0; mt < 2; ++mt) {
        float a[4];
#pragma unroll
        for (int r = 0; r < 4; ++r) {
          float g = fmaf(acc[f][mt][r], rs[mt], fmaf(tm[mt], cs[r], bia[r]));
          float s1 = __builtin_amdgcn_exp2f(g) + 1.f;
          float rr = __builtin_amdgcn_rcpf(fmaf(s1, s1, 1.f));
          a[r] = g * fmaf(rr, -2.f * LN2, LN2);
        }
        pk[f & 1][mt] = u32x2{pkbf(a[0], a[1]), pkbf(a[2], a[3])};
        if constexpr (f < 2) {
          SB();
          const int n = 2 * f + mt;
          const u32 so = 9 * 16384 + n * 8192, dd = sbase + 3 * STAGE + 4096 * w + n * 1024;
          dma16(sw2, w2o, so, dd); dma16(sw2, w2o, so + 896 * 1024, dd + 16384);
          SB();
        }
      }
      if constexpr (f & 1) {
#pragma unroll
        for (int mt = 0; mt < 2; ++mt)
          __builtin_amdgcn_raw_buffer_store_b128(cat(pk[0][mt], pk[1][mt]), rs_ws, abase + (f >> 1) * 1024, mt * 56 * 1024, 16);
      }
      if constexpr (f == 6) {
#pragma unroll
        for (int mt = 0; mt < 2; ++mt)
          __builtin_amdgcn_raw_buffer_store_b64(pk[0][mt], rs_ws, abase + (jp ? -1024 + 8 : 3072), mt * 56 * 1024, 16);
      }
    });
    WAITV(6);
    ARRIVE(E_W2);
    w2_regs();
    WAITV(0);
    if (l == 0) __hip_atomic_store((GAS u64*)(ws + WS_FACT + ((bi * 16 + bj) * 8 + q4) * FSTR), tag, RLX_AGENT);
    __builtin_amdgcn_s_setprio(0);
    w2_req();
  } else {
    const int ts = t - 256;
    __builtin_amdgcn_s_setprio(S_PRIO);
    WAITV(8);
    asm volatile("" : "+v"(p_ar), "+v"(p_ai), "+v"(p_br), "+v"(p_bi), "+v"(p_cr), "+v"(p_ci));
    asm volatile("" : "+v"(sr0), "+v"(sr1));
    {
      float e[16];
#pragma unroll
      for (int q = 0; q < 4; ++q) { e[2 * q] = bflo(sr0[q]); e[2 * q + 1] = bfhi(sr0[q]); e[8 + 2 * q] = bflo(sr1[q]); e[9 + 2 * q] = bfhi(sr1[q]); }
      float s0 = (e[0] + e[1]) + (e[2] + e[3]), s1 = (e[4] + e[5]) + (e[6] + e[7]), s2 = (e[8] + e[9]) + (e[10] + e[11]), s3 = (e[12] + e[13]) + (e[14] + e[15]);
      const float mean = half_sum((s0 + s1) + (s2 + s3)) * (1.0f / D_);
      float v0 = 0, v1 = 0;
#pragma unroll
      for (int q = 0; q < 8; ++q) { float d0 = e[q] - mean, d1 = e[8 + q] - mean; v0 = fmaf(d0, d0, v0); v1 = fmaf(d1, d1, v1); }
      const float var = half_sum(v0 + v1) * (1.0f / D_);
      const float sd = __builtin_amdgcn_sqrtf(rbf(rbf(var) + 1e-5f));
      const u32 pay = pkbf(mean, sd);
      if ((l & 31) == 0) __hip_atomic_store((GAS u64*)(ws + WS_STAT + (128 * bi + 8 * bj + 2 * q4 + (l >> 5)) * 8), ((u64)(u32)tag << 32) | pay, RLX_AGENT);
    }
    WAITV(1);
    ARRIVE(C_LANDED + 2);
    u32x4 xs0 = ald16(sx, (128 * bi + (ts >> 1)) * 1024 + bj * 64 + (ts & 1) * 32);
    u32x4 xs1 = ald16(sx, (128 * bi + (ts >> 1)) * 1024 + bj * 64 + (ts & 1) * 32 + 16);
    u32 b1w = ald4(mk_srd(P.b1, F_ * 2), bj * 224 + 4 * l);
    float yscale, gmax, zr, zi;
    cpx cbn;
    {
      const float LOG2E = 1.4426950408889634f, INV2PI = 0.15915494309189535f;
      const float lx = cc ? bfhi(ldt2) : bflo(ldt2);
      const float th = lx * LOG2E, tl = fmaf(lx, LOG2E, -th) + lx * 1.925963033500011e-8f;
      const float dt = rbf(__builtin_amdgcn_exp2f(th) * fmaf(tl, 0.6931471805599453f, 1.0f));
      cpx cb0 = cmul(cpx{bflo(p_cr), bflo(p_ci)}, cpx{dt * bflo(p_br), dt * bflo(p_bi)});
      cpx cb1 = cmul(cpx{bfhi(p_cr), bfhi(p_ci)}, cpx{dt * bfhi(p_br), dt * bfhi(p_bi)});
      const float z0 = dt * bflo(p_ar) * LOG2E, z1 = dt * bfhi(p_ar) * LOG2E;
      gmax = fmaxf(wave_max(fmaxf(z0, z1)), 0.f);
      const float cbmax = wave_max(fmaxf(fmaxf(fabsf(cb0.re), fabsf(cb0.im)), fmaxf(fabsf(cb1.re), fabsf(cb1.im))));
      const float cbs = __uint_as_float((__float_as_uint(cbmax) + 0x007fffffu) & 0x7f800000u);
      const float icb = cbs > 0.f ? __builtin_amdgcn_rcpf(cbs) : 0.f;
      yscale = cbs * __builtin_amdgcn_exp2f(gmax * 2048.f) * (1.0f / 64.f);
      zr = fmaxf((hm ? z1 : z0) - gmax, -1e30f);
      zi = dt * (hm ? bfhi(p_ai) : bflo(p_ai)) * INV2PI;
      cbn = hm ? cb1 : cb0;
      cbn.re *= 64.f * icb; cbn.im *= -64.f * icb;
    }
    auto cexp = [&](float a) {
      float mg = __builtin_amdgcn_exp2f(zr * a);
      float ph = __builtin_amdgcn_fractf(zi * a);
      return cpx{mg * __builtin_amdgcn_cosf(ph), mg * __builtin_amdgcn_sinf(ph)};
    };
    const cpx e1 = cexp(1.f), e8 = cexp(8.f), e128 = cexp(128.f);
    GAS u64* stat_p = (GAS u64*)(ws + WS_STAT + (128 * bi + (ts >> 1)) * 8);
    u64 stat_v = 0;
    LAS char* tb = opq(S + KT + q4 * KTW + l * 4);
    LAS char* tf = opq(S + KT + q4 * KTW + l15 * 272 + h * 16);
    auto put = [&](int row, cpx v) { *(volatile LAS u32*)(tb + row * 272) = pkh(v.re, v.im); };
    u32x4 fa[4];
    f32x4 kacc[3];
    auto tile_mma = [&]() {
      f32x4 a = {0, 0, 0, 0};
#pragma unroll
      for (int k = 0; k < 4; ++k) a = mfma_h(fa[k], *(volatile LAS u32x4*)(tf + k * 64), a);
      return a;
    };
    {
      cpx wv = cbn;
      const cpx e8c = cpx{e8.re, -e8.im};
#pragma unroll
      for (int q = 0; q < 16; ++q) { put(q, wv); if (q < 15) wv = cmul(wv, e8c); if (q == 6) stat_v = probe(stat_p); }
#pragma unroll
      for (int k = 0; k < 4; ++k) fa[k] = *(volatile LAS u32x4*)(tf + k * 64);
    }
    if (!__all((u32)(stat_v >> 32) == (u32)tag)) {
#pragma unroll 1
      for (int it = 0; it < SPIN_CAP; ++it) {
        __builtin_amdgcn_s_sleep(16);
        stat_v = probe(stat_p);
        if (__all((u32)(stat_v >> 32) == (u32)tag)) break;
      }
    }
    WAITV(0);
    asm volatile("" : "+v"(xs0), "+v"(xs1), "+v"(b1w));
    {
      const u32 pay = (u32)stat_v;
      const float mb = bflo(pay), rsd = __builtin_amdgcn_rcpf(bfhi(pay));
      *(LAS u32*)(S + STO + (ts >> 1) * 4) = pay;
      *(LAS u32*)(S + B1O + l * 4) = b1w;
      LAS char* xt = opq(S + XT + (ts & 1) * 16 * 272 + (ts >> 1) * 2);
#pragma unroll
      for (int q = 0; q < 8; ++q) {
        const u32 xv = q < 4 ? xs0[q] : xs1[q - 4];
        u32 dd = pkbf(bflo(xv) - mb, bfhi(xv) - mb);
        u32 xn = pkbf(bflo(dd) * rsd, bfhi(dd) * rsd);
        *(LAS u16*)(xt + (2 * q) * 272) = (u16)xn;
        *(LAS u16*)(xt + (2 * q + 1) * 272) = (u16)(xn >> 16);
      }
      ARRIVE(E_XT);
    }
    cpx e2, e4, e6;
    {
      cpx lo = cpx{1.f, 0.f}, g = e128;
#pragma unroll
      for (int q = 0; q < 8; ++q) {
        put(q, lo); put(8 + q, g);
        if (q == 2) e2 = lo;
        if (q == 4) e4 = lo;
        if (q == 6) e6 = lo;
        if (q < 7) { lo = (q == 0) ? e1 : cmul(lo, e1); g = cmul(g, e1); }
        if (q == 1) {
          AWAIT(E_XT);
          LAS char* xr = opq(S + XT + (ts >> 4) * 272 + (ts & 15) * 16);
          u32x4 v0 = *(LAS u32x4*)(xr), v1 = *(LAS u32x4*)(xr + 16 * 272);
          const u32 xo = WS_XNT + ((32 * bj + (ts >> 4)) * 2048 + 128 * bi + (ts & 15) * 8) * 2;
          __builtin_amdgcn_raw_buffer_store_b128(v0, rs_ws, xo, 0, 16);
          __builtin_amdgcn_raw_buffer_store_b128(v1, rs_ws, xo, 16 * 4096, 16);
          ARRIVE(E_XTR);
        }
      }
      kacc[0] = tile_mma();
    }
    cpx hv = cmul(e128, e128);
#pragma unroll
    for (int a = 2; a < 8; ++a) {
      if (a < 4) {
        put((a - 2) * 4 + 0, hv); put((a - 2) * 4 + 1, cmul(hv, e2));
        put((a - 2) * 4 + 2, cmul(hv, e4)); put((a - 2) * 4 + 3, cmul(hv, e6));
      } else {
        put(8 + (a - 4) * 2 + 0, hv); put(8 + (a - 4) * 2 + 1, cmul(hv, e4));
      }
      hv = cmul(hv, e128);
      if (a == 3) {
        WAITV(0);
        if (l == 0) __hip_atomic_store((GAS u64*)(ws + WS_FXN + ((bj * 16 + bi) * 4 + q4) * FSTR), tag, RLX_AGENT);
      }
    }
    kacc[1] = tile_mma();
#pragma unroll
    for (int a = 8; a <= 16; ++a) { put(a - 8, hv); if (a < 16) hv = cmul(hv, e128); }
    GAS u64* fxn_p = (GAS u64*)(ws + WS_FXN + (((ch >> 5) * 16 + l15) * 4 + ((ch & 15) >> 2)) * FSTR);
    u64 fxn_v;
    kacc[2] = tile_mma();
    AWAIT(E_XTR);
    {
      LAS char* kx = opq(S + KX + q4 * 2048 + l * 16);
      *(LAS f32x4*)(kx) = hm ? kacc[0] : kacc[1];
      *(LAS f32x4*)(kx + 1024) = kacc[2];
      ARRIVE(E_KX);
      fxn_v = probe(fxn_p);
      const int e = hm ? 1 + (l15 >> 3) : 0, lg = 3 - e, r8 = hm ? (l15 & 7) : l15;
      const int idx = (e ? 128 + 128 * e : 0) + ((r8 >> lg) << (7 - e)) + (r8 & ((1 << lg) - 1)) + ((4 * h) << lg);
      const int id2 = min(512 + 16 * l15 + 4 * h + 2 * hm, 642);
      LAS char* kl = S + KLIN + cc * KLS;
      AWAIT(E_KX);
      LAS char* ko_ = opq(S + KX + (q4 ^ 1) * 2048 + l * 16);
      f32x4 ka = (hm ? kacc[1] : kacc[0]) + *(LAS f32x4*)(ko_);
      f32x4 kb = kacc[2] + *(LAS f32x4*)(ko_ + 1024);
      float k2a = hm ? kb[2] : kb[0], k2b = hm ? kb[3] : kb[1];
      if (gmax > 0.f) {
#pragma unroll
        for (int r = 0; r < 4; ++r) ka[r] *= __builtin_amdgcn_exp2f(gmax * (float)(((idx + (r << lg) - 128 * e) << e) - 2048));
        k2a *= __builtin_amdgcn_exp2f(gmax * (float)(((id2 - 384) << 3) - 2048));
        k2b *= __builtin_amdgcn_exp2f(gmax * (float)(((id2 - 383) << 3) - 2048));
      }
#pragma unroll
      for (int r = 0; r < 4; ++r) *(LAS _Float16*)(kl + (idx + (r << lg)) * 2) = (_Float16)ka[r];
      *(LAS u32*)(kl + id2 * 2) = pkh(k2a, k2b);
    }
    ARRIVE(E_KLIN);
    const int tc = hm * 64 + l;
    wait_flags(fxn_p, fxn_v, tag);
    u32x2 us[4];
    const u32 yo = (ch * 2048 + 16 * l15 + 4 * h) * 2;
    const u32 t0 = hm ? 512u : 0u, t1 = hm ? 1024u : 1536u, t2 = hm ? 2560u : 2048u, t3 = hm ? 3072u : 3584u;
    u32x4 ua, ub; u32 uc;
    {
      const u32 uo = WS_XNT + (ch * 2048 + tc * 16) * 2;
      ua = __builtin_amdgcn_raw_buffer_load_b128(rs_ws, uo, 0, 16);
      ub = __builtin_amdgcn_raw_buffer_load_b128(rs_ws, uo + 16, 0, 16);
      uc = __builtin_amdgcn_raw_buffer_load_b32(rs_ws, uo + 32, 0, 16);
      us[0] = __builtin_amdgcn_raw_buffer_load_b64(rs_ws, yo, WS_XNT + t0, 16);
      us[1] = __builtin_amdgcn_raw_buffer_load_b64(rs_ws, yo, WS_XNT + t1, 16);
      us[2] = __builtin_amdgcn_raw_buffer_load_b64(rs_ws, yo, WS_XNT + t2, 16);
      us[3] = __builtin_amdgcn_raw_buffer_load_b64(rs_ws, yo, WS_XNT + t3, 16);
    }
    AWAIT(E_KLIN);
    {
      LAS char* kl = S + KLIN + cc * KLS;
      const float jf = (float)(2047 - 16 * tc);
      const bool low = tc >= 120;
      u32 ko[8];
#pragma unroll
      for (int q = 0; q < 8; ++q) {
        const u32 bits = pkbf(jf - (float)(2 * q), jf - (float)(2 * q + 1));
        const u32 j0 = 2047 - 16 * tc - 2 * q;
        const u32 pr = low ? (j0 | ((j0 - 1) << 16)) : bits - 0x42804280u;
        ko[q] = (u32)*(LAS u16*)(kl + (pr & 0xffff) * 2) | ((u32)*(LAS u16*)(kl + (pr >> 16) * 2) << 16);
      }
      LAS char* kr = opq(S + KRO + cc * KRS + tc * 32);
      *(LAS u32x4*)(kr) = u32x4{ko[0], ko[1], ko[2], ko[3]};
      *(LAS u32x4*)(kr + 16) = u32x4{ko[4], ko[5], ko[6], ko[7]};
      *(LAS u32x4*)(S + KRO + cc * KRS + 4096 + min(tc, 29) * 16) = u32x4{0, 0, 0, 0};
    }
    {
      u32 d[9], o[8];
#pragma unroll
      for (int q = 0; q < 4; ++q) { d[q] = pkh(bflo(ua[q]), bfhi(ua[q])); d[4 + q] = pkh(bflo(ub[q]), bfhi(ub[q])); }
      d[8] = pkh(bflo(uc), 0.f);
#pragma unroll
      for (int q = 0; q < 8; ++q) o[q] = __builtin_amdgcn_alignbit(d[q + 1], d[q], 16);
      LAS char* ucb = opq(S + UC + cc * 2 * UCS + (2 * tc + 2) * 16);
#pragma unroll
      for (int k = 0; k < 2; ++k) {
        *(LAS u32x4*)(ucb + k * 16) = u32x4{d[4 * k], d[4 * k + 1], d[4 * k + 2], d[4 * k + 3]};
        *(LAS u32x4*)(ucb + k * 16 + UCS) = u32x4{o[4 * k], o[4 * k + 1], o[4 * k + 2], o[4 * k + 3]};
      }
      if (tc == 0) {
        LAS char* z = ucb - 32;
        *(LAS u32x4*)(z) = u32x4{0, 0, 0, 0};
        *(LAS u32x4*)(z + UCS) = u32x4{0, 0, 0, 0};
        *(LAS u32x4*)(z + 16) = u32x4{0, 0, 0, 0};
        *(LAS u32x4*)(z + UCS + 16) = u32x4{0, 0, 0, d[0] << 16};
      }
    }
    ARRIVE(E_UC);
    AWAIT(E_UC);
    {
#pragma unroll
      for (int q = 0; q < 4; ++q) ca[q] = f32x4{0, 0, 0, 0};
      const int v = l15 + 1, sft = v & 1;
      LAS char* ub0 = S + UC + (cc * 2 + sft) * UCS + (8 * h + v - sft) * 2;
      LAS char* ub[4];
#pragma unroll
      for (int q = 0; q < 4; ++q) ub[q] = opq(ub0 + q * 1024 - 64);
      LAS char* kbase = opq(S + KRO + cc * KRS + (2032 - 1792 - 16 * l15 + 8 * h) * 2);
      auto cv = [&](auto Gc) {
        constexpr int G = CV(Gc);
        constexpr int TA = G ? 1 : 0, TB = G ? 2 : 3, TC = G ? 5 : 4, TD = G ? 6 : 7;
        u32x4 U[2][TD + 1], Kf[2][TD + 1];
        auto ldu = [&](int i, int r) {
          LAS u32* p = (LAS u32*)(ub[i >> 1] + (i & 1) * 512 + r * 64);
          return u32x4{p[0], p[1], p[2], p[3]};
        };
#pragma unroll
        for (int i = 0; i <= TD; ++i) { U[0][i] = ldu(i, 1); Kf[0][TD - i] = *(LAS u32x4*)(kbase + (7 - (TD - i)) * 512); }
#pragma unroll 1
        for (int rp = 0; rp < 4; ++rp) {
          static_for<0, 2>([&](auto pc) {
            constexpr int p = CV(pc);
            static_for<0, TD + 1>([&](auto ic) {
              constexpr int i = CV(ic);
              SB();
              ca[3] = mfma_h(U[p][i], Kf[p][TD - i], ca[3]);
              SB();
              U[p ^ 1][i] = ldu(i, p + 2);
              SB();
              if constexpr (i >= TD - TC) ca[2] = mfma_h(U[p][i - (TD - TC)], Kf[p][TD - i], ca[2]);
              SB();
              Kf[p ^ 1][TD - i] = *(LAS u32x4*)(kbase + (p + 1) * 64 + (7 - (TD - i)) * 512);
              SB();
              if constexpr (i >= TD - TB) ca[1] = mfma_h(U[p][i - (TD - TB)], Kf[p][TD - i], ca[1]);
              if constexpr (i >= TD - TA) ca[0] = mfma_h(U[p][i - (TD - TA)], Kf[p][TD - i], ca[0]);
            });
          });
          SB();
#pragma unroll
          for (int q = 0; q < 4; ++q) ub[q] = opq(ub[q] + 128);
          kbase = opq(kbase + 128);
        }
      };
      if (hm == 0) cv(std::integral_constant<int, 0>{}); else cv(std::integral_constant<int, 1>{});
    }
    const float dsk = cc ? bfhi(dsk2) : bflo(dsk2);
    u32 tsk[4][2];
#pragma unroll
    for (int q = 0; q < 4; ++q) {
      tsk[q][0] = pkbf(bflo(us[q][0]) * dsk, bfhi(us[q][0]) * dsk);
      tsk[q][1] = pkbf(bflo(us[q][1]) * dsk, bfhi(us[q][1]) * dsk);
    }
    ARRIVE(E_CONV);
#pragma unroll
    for (int q = 0; q < 4; ++q) {
      const f32x4 a = ca[q];
      u32 y01 = pkbf(a[0] * yscale, a[1] * yscale), y23 = pkbf(a[2] * yscale, a[3] * yscale);
      u32x2 o = {pkbf(bflo(y01) + bflo(tsk[q][0]), bfhi(y01) + bfhi(tsk[q][0])), pkbf(bflo(y23) + bflo(tsk[q][1]), bfhi(y23) + bfhi(tsk[q][1]))};
      __builtin_amdgcn_raw_buffer_store_b64(o, rs_ws, yo, WS_YT + (q == 0 ? t0 : q == 1 ? t1 : q == 2 ? t2 : t3), 16);
    }
    AWAIT(E_W2);
    w2_regs();
    WAITV(0);
    if (l == 0) __hip_atomic_store((GAS u64*)(ws + WS_FY + (b * 4 + q4) * FSTR), tag, RLX_AGENT);
    if (b == 0 && w == 7) {
      GAS u64* p = (GAS u64*)(ws + WS_FXN + (size_t)l * 4 * FSTR);
      const u64 v0 = probe(p), v1 = probe(p + 64 * 4 * FSTR / 8), v2 = probe(p + 128 * 4 * FSTR / 8), v3 = probe(p + 192 * 4 * FSTR / 8);
      wait_flags(p, v0, tag); wait_flags(p + 64 * 4 * FSTR / 8, v1, tag);
      wait_flags(p + 128 * 4 * FSTR / 8, v2, tag); wait_flags(p + 192 * 4 * FSTR / 8, v3, tag);
      if (l == 0) __hip_atomic_store((GAS u64*)ws, tag, RLX_AGENT);
    }
    __builtin_amdgcn_s_setprio(0);
  }

  {
    GAS u64* fact_p = (GAS u64*)(ws + WS_FACT + (((bp >> 1) * 16 + 2 * (w | (l & 4)) + (l & 1)) * 8 + 2 * (bp & 1) + ((l >> 1) & 1)) * FSTR);
    wait_flags(fact_p, probe(fact_p), tag);
    const u32 ao = WS_ACT + (4 * bp * 56 + 7 * w) * 1024 + l * 16;
    u32x4 af[4][4];
#define KORD(i) ((i) < 2 ? (i) : (i) < 4 ? (i) + 2 : (i) < 6 ? (i) - 2 : 6)
#define KPOS(s) ((s) < 7 ? KORD(s) : 28 + KORD((s) - 4))
    auto lda = [&](auto sc, auto lc) {
      af[CV(sc) & 3][CV(lc)] = __builtin_amdgcn_raw_buffer_load_b128(rs_ws, ao, (CV(lc) * 56 + KPOS(CV(sc))) * 1024, 16);
    };
    static_for<0, 3>([&](auto sc) { static_for<0, 4>([&](auto lc) { lda(sc, lc); }); });
    f32x4 ac[4][4];
#pragma unroll
    for (int dt = 0; dt < 4; ++dt)
#pragma unroll
      for (int lt = 0; lt < 4; ++lt) ac[dt][lt] = f32x4{0, 0, 0, 0};
    const int yT = bp >> 2;
    GAS u64* fy_p = (GAS u64*)(ws + WS_FY + ((32 * bc + 8 * q4 + ((l >> 1) & 7)) * 4 + (l & 1) * 2 + ((yT ^ (yT >> 1)) & 1)) * FSTR);
    u64 fy_v = 0;
    u32x4 xres = __builtin_amdgcn_raw_buffer_load_b128(rs_x, (64 * bp + (t >> 3)) * 1024 + bc * 128 + (t & 7) * 16, 0, 0);
    auto step = [&](auto sc, auto nc) {
      constexpr int s = CV(sc), NS = CV(nc);
      u32x4 wf[4];
#pragma unroll
      for (int dt = 0; dt < 4; ++dt) {
        if constexpr (s < 4) wf[dt] = wr1[s][dt];
        else if constexpr (s < 7) wf[dt] = w2_frag(dt, s - 4);
        else if constexpr (s == 7) wf[dt] = w2_frag(dt, 3, 16384);
        else if constexpr (s == 8) wf[dt] = w2_frag(dt, 3);
        else wf[dt] = w2_frag(dt, s - 9);
      }
      static_for<0, 4>([&](auto lc) {
        constexpr int lt = CV(lc);
        SB();
#pragma unroll
        for (int dt = 0; dt < 4; ++dt) ac[dt][lt] = mfma_bf(wf[dt], af[s & 3][lt], ac[dt][lt]);
        SB();
        if constexpr (s == 4) ISSUE_W2P(3, lt, 0);
        if constexpr (s == 5) ISSUE_W2P(6, lt, 1);
        if constexpr (s + 3 < NS) lda(std::integral_constant<int, s + 3>{}, lc);
      });
    };
    if (w < 4) {
      static_for<0, 11>([&](auto sc) { step(sc, std::integral_constant<int, 11>{}); });
    } else {
      fy_v = probe(fy_p);
      static_for<0, 3>([&](auto sc) { step(sc, std::integral_constant<int, 3>{}); });
    }
    u32x4 y0, y1;
    if (w >= 4) {
      wait_flags(fy_p, fy_v, tag);
      const u32 yoff = WS_YT + ((64 * bc + 16 * q4 + (l >> 3)) * 2048 + 64 * bp + (l & 7) * 8) * 2;
      y0 = __builtin_amdgcn_raw_buffer_load_b128(rs_ws, yoff, 0, 16);
      y1 = __builtin_amdgcn_raw_buffer_load_b128(rs_ws, yoff, 8 * 4096, 16);
    }
    if (w < 4) ARRIVE(E_G2);
    AWAIT(E_CONV);
#pragma unroll
    for (int dt = 0; dt < 4; ++dt) {
      const int sg = ((2 * dt + (h >> 1)) & 3) * 4;
      if (dt == 3 && w >= 4) AWAIT(E_G2);
      LAS char* pw = opq(S + dt * STAGE + 4096 * w + h * 256 + ((l15 ^ sg) * 16));
#pragma unroll
      for (int lt = 0; lt < 4; ++lt) *(LAS f32x4*)(pw + lt * 1024) = ac[dt][lt];
    }
    if (w >= 4) {
      LAS char* yw = opq(S + YI + (16 * q4 + (l >> 3)) * 128 + (((l & 7) ^ (2 * q4)) * 16));
      *(LAS u32x4*)(yw) = y0;
      *(LAS u32x4*)((LAS char*)((size_t)yw ^ 16) + 1024) = y1;
    }
    BAR();
    {
      const int lrow = t >> 3, g = t & 7;
      LAS char* pr = opq(S + (g >> 1) * STAGE + (lrow >> 4) * 1024 + (2 * (g & 1)) * 256 + (((lrow & 15) ^ ((g & 3) * 4)) * 16));
      LAS char* yr = opq(S + YI + 8 * g * 128 + (((lrow >> 3) ^ g) * 16) + (lrow & 7) * 2);
      f32x4 s0 = *(LAS f32x4*)(pr), s1 = *(LAS f32x4*)(pr + 256);
#pragma unroll
      for (int p = 1; p < 8; ++p) {
        s0 += *(LAS f32x4*)(pr + 4096 * p);
        s1 += *(LAS f32x4*)(pr + 4096 * p + 256);
      }
      float yb[8] = {s0[0], s0[1], s0[2], s0[3], s1[0], s1[1], s1[2], s1[3]};
      u32 o[4];
#pragma unroll
      for (int q = 0; q < 4; ++q) {
        u32 y0 = *(LAS u16*)(yr + (2 * q) * 128);
        u32 y1 = *(LAS u16*)(yr + (2 * q + 1) * 128);
        u32 a = pkbf(bflo(xres[q]) + bflo(y0), bfhi(xres[q]) + bflo(y1));
        u32 bq = pkbf(yb[2 * q], yb[2 * q + 1]);
        o[q] = pkbf(bflo(a) + bflo(bq), bfhi(a) + bfhi(bq));
      }
      __builtin_amdgcn_raw_buffer_store_b128(u32x4{o[0], o[1], o[2], o[3]}, rs_out, (64 * bp + lrow) * 1024 + bc * 128 + g * 16, 0, 0);
    }
  }
}

#if defined(__HIP_DEVICE_COMPILE__)
#pragma clang attribute pop
#endif

extern "C" void kernel_launch(void* const* d_in, const int* in_sizes, int n_in, void* d_out, int out_size, void* d_ws,
                              size_t ws_size, hipStream_t stream) {
  Params P;
  P.x = (const u16*)d_in[0];
  P.Are = (const u16*)d_in[1];
  P.Aim = (const u16*)d_in[2];
  P.Bre = (const u16*)d_in[3];
  P.Bim = (const u16*)d_in[4];
  P.Cre = (const u16*)d_in[5];
  P.Cim = (const u16*)d_in[6];
  P.logdt = (const u16*)d_in[7];
  P.dskip = (const u16*)d_in[8];
  P.w1 = (const u16*)d_in[10];
  P.b1 = (const u16*)d_in[11];
  P.w2 = (const u16*)d_in[12];
  P.out = (u16*)d_out;
  P.ws = (char*)d_ws;
  (void)hipFuncSetAttribute((const void*)fused_kernel, hipFuncAttributeMaxDynamicSharedMemorySize, LDS_BYTES);
  fused_kernel<<<256, 512, LDS_BYTES, stream>>>(P);
}
```
